```python
import torch

def _softmax(x, axis=-1):
    return torch.softmax(x, dim=axis)
def _gelu(x):
    return torch.nn.functional.gelu(x, approximate="tanh")
def _silu(x):
    return torch.nn.functional.silu(x)
def _sigmoid(x):
    return torch.sigmoid(x)
def _layernorm(x, axis=-1, eps=1e-5):
    m = x.mean(dim=axis, keepdim=True)
    v = x.var(dim=axis, keepdim=True, unbiased=False)
    return (x - m) / torch.sqrt(v + eps)
def _scatter_add(tgt, idx, src, axis=0):
    return tgt.clone().index_add_(axis, idx, src)
def _segment_sum(src, seg, n):
    out = torch.zeros((n,) + src.shape[1:], dtype=src.dtype)
    return out.index_add_(0, seg, src)
def _pad(x, pw):
    flat = tuple(v for pair in reversed(pw) for v in pair)
    return torch.nn.functional.pad(x, flat)

kernel_name = 'concat_heads__gqa_block_cached__compressed_kv_cla__decode'

def setup_inputs():

    _w = (lambda a: (lambda t: t.to(torch.bfloat16) if t.is_floating_point() else t)(torch.as_tensor(a)))
    def _A_setup():
        B, Sq, D = 2, 1, 512
        Hq, Hkv, Sk, Dh = 32, 32, 8192, 128
        assert Hq % Hkv == 0, (Hq, Hkv)
        return {
            "x": _w((lambda *s: torch.randn(*s, dtype=torch.bfloat16) * 1.6)(B, Sq, D)),
            "k_cache": _w((lambda *s: torch.randn(*s, dtype=torch.bfloat16) * 1.6)(B, Hkv, Sk, Dh)),
            "v_cache": _w((lambda *s: torch.randn(*s, dtype=torch.bfloat16) * 0.05)(B, Hkv, Sk, Dh)),
            "wq": _w((lambda *s: torch.randn(*s, dtype=torch.bfloat16) * 1.6)(D, Hq * Dh) * (D ** -0.5)),
            "wo": _w((lambda *s: torch.randn(*s, dtype=torch.bfloat16) * 0.05)(Hq * Dh, D) * ((Hq * Dh) ** -0.5)),
        }
    _A_in = _A_setup()

    def _B_setup():
        B, H, Sq, Sk, D, M = 2, 32, 1, 8192, 128, 512
        Dc = 4 * D
        return {
            "x": _w((lambda *s: torch.randn(*s, dtype=torch.bfloat16) * 3.2)(B, Sq, M)),
            "c_kv": _w((lambda *s: torch.randn(*s, dtype=torch.bfloat16) * 0.1)(B, Sk, Dc)),
            "w_q": _w((lambda *s: torch.randn(*s, dtype=torch.bfloat16) * 6.4)(M, H, D) * (M ** -0.5)),
            "w_uk": _w((lambda *s: torch.randn(*s, dtype=torch.bfloat16) * 6.4)(Dc, H, D) * (Dc ** -0.5)),
            "w_uv": _w((lambda *s: torch.randn(*s, dtype=torch.bfloat16) * 0.1)(Dc, H, D) * (Dc ** -0.5)),
            "w_o": _w((lambda *s: torch.randn(*s, dtype=torch.bfloat16) * 0.1)(H, D, M) * ((H * D) ** -0.5)),
        }
    _B_in = _B_setup()

    return {
        "A_x": _A_in["x"],
        "A_k_cache": _A_in["k_cache"],
        "A_v_cache": _A_in["v_cache"],
        "A_wq": _A_in["wq"],
        "A_wo": _A_in["wo"],
        "B_x": _B_in["x"],
        "B_c_kv": _B_in["c_kv"],
        "B_w_q": _B_in["w_q"],
        "B_w_uk": _B_in["w_uk"],
        "B_w_uv": _B_in["w_uv"],
        "B_w_o": _B_in["w_o"],
    }

def reference(A_x, A_k_cache, A_v_cache, A_wq, A_wo, B_x, B_c_kv, B_w_q, B_w_uk, B_w_uv, B_w_o):
    def _A_ref(x, k_cache, v_cache, wq, wo):
        B, Sq, D = x.shape
        Hq, Dh = 32, 128
        Hkv = k_cache.shape[1]
        rep = Hq // Hkv
        q = (lambda x, *d: x.permute(*d))((x @ wq).reshape(B, Sq, Hq, Dh), 0, 2, 1, 3)
        k = (lambda x, n, axis: torch.repeat_interleave(x, n, dim=axis))(k_cache, rep, axis=1)
        v = (lambda x, n, axis: torch.repeat_interleave(x, n, dim=axis))(v_cache, rep, axis=1)
        scores = torch.einsum("bhqd,bhkd->bhqk", q, k) * (Dh ** -0.5)
        p = _softmax(scores, axis=-1)
        o = torch.einsum("bhqk,bhkd->bhqd", p, v)
        return (lambda x, *d: x.permute(*d))(o, 0, 2, 1, 3).reshape(B, Sq, Hq * Dh) @ wo

    def _B_ref(x, c_kv, w_q, w_uk, w_uv, w_o):
        B, Sq, M = x.shape[0], x.shape[1], x.shape[2]
        Sk = c_kv.shape[1]
        H, D = w_q.shape[1], w_q.shape[2]
        q = torch.einsum("bqm,mhd->bhqd", x, w_q)
        k = torch.einsum("bkc,chd->bhkd", c_kv, w_uk)
        v = torch.einsum("bkc,chd->bhkd", c_kv, w_uv)
        scores = torch.einsum("bhqd,bhkd->bhqk", q, k) * (D ** -0.5)
        off = Sk - Sq
        qi = torch.arange(Sq)[:, None]
        kj = torch.arange(Sk)[None, :]
        cmask = kj <= (qi + off)
        scores = torch.where(cmask[None, None], scores, -1e9)
        p = _softmax(scores, axis=-1)
        ctx = torch.einsum("bhqk,bhkd->bhqd", p, v)
        return torch.einsum("bhqd,hdm->bqm", ctx, w_o)

    a = _A_ref(A_x, A_k_cache, A_v_cache, A_wq, A_wo)
    b = _B_ref(B_x, B_c_kv, B_w_q, B_w_uk, B_w_uv, B_w_o)
    return torch.concatenate([a, b], axis=-1)
```

```cpp
#include <hip/hip_runtime.h>
#include <hip/hip_bf16.h>
#include <stdint.h>
#include <type_traits>

typedef unsigned short u16;
typedef unsigned int u32;
typedef unsigned long long u64;
using u32x2  = __attribute__((ext_vector_type(2))) u32;
using u32x4  = __attribute__((ext_vector_type(4))) u32;
using f32x2  = __attribute__((ext_vector_type(2))) float;
using f32x4  = __attribute__((ext_vector_type(4))) float;
using f32x16 = __attribute__((ext_vector_type(16))) float;
using s16x4  = __attribute__((ext_vector_type(4))) short;
using s16x8  = __attribute__((ext_vector_type(8))) short;
using bf16x2 = __attribute__((ext_vector_type(2))) __bf16;
using bf16x8 = __attribute__((ext_vector_type(8))) __bf16;
#define LAS __attribute__((address_space(3)))
#define DEVI __device__ __forceinline__
#define RLX_AGENT __ATOMIC_RELAXED, __HIP_MEMORY_SCOPE_AGENT

constexpr float SCALE = 0.088388347648318440f;
constexpr float LOG2E = 1.4426950408889634f;
constexpr int SK = 8192, DH = 128, DM = 512, DC = 512;

constexpr u32 FL_Q = 0, FL_B = 4096, FL_TMO = 8192;
constexpr u32 WS_MA   = 12288;
constexpr u32 VOID = 0xffffffffu;
constexpr u32 WS_QB   = WS_MA + 128 * 1024;
constexpr u32 WS_QF   = WS_QB + 32 * 1024;
constexpr u32 WS_LML  = WS_QF + 2 * 32 * 2 * 512 * 2;
constexpr u32 WS_LATP = WS_LML + 32 * 32 * 2 * 4;
constexpr u32 WS_END  = WS_LATP + 32 * 32 * 512 * 2;
constexpr u32 OOB = 0x80000000u;
constexpr u32 SPIN_CAP = 1u << 17;
#define PSTAMP(i) do { } while (0)
#define PDUMP() do { } while (0)
#define STAMP(i) do { } while (0)
#define LSTAMP(i) do { } while (0)
#define WSTAMP(i) do { } while (0)

constexpr int L_TOTAL = 163840;
constexpr int LA_Q   = 0;
constexpr int LA_ML  = 256;
constexpr int LA_ACC = 512;
constexpr int LA_SC  = 8192;
constexpr int LC_RING = 0;
constexpr int LC_SP   = 131072;
constexpr int LC_MX   = LC_SP;
constexpr int LC_P    = LC_SP + 512;
constexpr int LC_LS   = LC_P + 4096;
constexpr int LH_WO   = 0;
constexpr int LH_RED  = 131072;
constexpr int LH_W    = LH_RED + 8192;
constexpr int LH_CTX  = LH_W + 256;
constexpr int LH_LAT  = LH_CTX + 512;
constexpr int LH_PART = LH_LAT + 4096;
constexpr int LH_Q    = LH_PART;
constexpr int LH_QS   = LH_Q + 512;
static_assert(LH_PART + 16384 <= L_TOTAL, "LDS");

DEVI float dot2(u32 a, u32 b, float c) {
  return __builtin_amdgcn_fdot2_f32_bf16(__builtin_bit_cast(bf16x2, a), __builtin_bit_cast(bf16x2, b), c, false);
}
DEVI u32 bf16bits(float x) { return (u32)__builtin_bit_cast(u16, (__bf16)x); }
DEVI float bf16r(float x) { return __builtin_bit_cast(float, bf16bits(x) << 16); }
DEVI float bflo(u32 v) { return __builtin_bit_cast(float, v << 16); }
DEVI float bfhi(u32 v) { return __builtin_bit_cast(float, v & 0xffff0000u); }
DEVI float asf(u32 v) { return __builtin_bit_cast(float, v); }
DEVI u32 asu(float v) { return __builtin_bit_cast(u32, v); }
DEVI u32 pack2(float lo, float hi) { return __builtin_bit_cast(u32, __builtin_convertvector(f32x2{lo, hi}, bf16x2)); }
template <int CTRL> DEVI float dpp(float x) {
  return __builtin_bit_cast(float, __builtin_amdgcn_mov_dpp(__builtin_bit_cast(int, x), CTRL, 0xf, 0xf, true));
}
template <int CTRL> DEVI u32 dppu(u32 x) { return (u32)__builtin_amdgcn_mov_dpp((int)x, CTRL, 0xf, 0xf, true); }
constexpr int XOR1 = 0xB1, XOR2 = 0x4E, HMIR = 0x141, ROR8 = 0x128, ROR4 = 0x124, BCAST = 0x150;
DEVI float xrow16_sum(float x) {
  auto s = __builtin_amdgcn_permlane16_swap(__float_as_uint(x), __float_as_uint(x), false, false);
  x = __uint_as_float(s[0]) + __uint_as_float(s[1]);
  auto t = __builtin_amdgcn_permlane32_swap(__float_as_uint(x), __float_as_uint(x), false, false);
  return __uint_as_float(t[0]) + __uint_as_float(t[1]);
}
DEVI float xrow16_max(float x) {
  auto s = __builtin_amdgcn_permlane16_swap(__float_as_uint(x), __float_as_uint(x), false, false);
  x = fmaxf(__uint_as_float(s[0]), __uint_as_float(s[1]));
  auto t = __builtin_amdgcn_permlane32_swap(__float_as_uint(x), __float_as_uint(x), false, false);
  return fmaxf(__uint_as_float(t[0]), __uint_as_float(t[1]));
}
DEVI float row16_sum(float x) { x += dpp<XOR1>(x); x += dpp<XOR2>(x); x += dpp<ROR4>(x); x += dpp<ROR8>(x); return x; }
DEVI float row16_max(float x) { x = fmaxf(x, dpp<XOR1>(x)); x = fmaxf(x, dpp<XOR2>(x)); x = fmaxf(x, dpp<ROR4>(x)); x = fmaxf(x, dpp<ROR8>(x)); return x; }
DEVI u32x4 ld_nt(const u16* p) { return __builtin_nontemporal_load(reinterpret_cast<const u32x4*>(p)); }
DEVI u32x4 ld16(const u16* p) { return *reinterpret_cast<const u32x4*>(p); }
DEVI u32 perm_lo(u32 a, u32 b) { return __builtin_amdgcn_perm(b, a, 0x05040100u); }
DEVI u32 perm_hi(u32 a, u32 b) { return __builtin_amdgcn_perm(b, a, 0x07060302u); }
DEVI f32x4 mfma16(const u32x4& a, const u32x4& b, const f32x4& c) {
  return __builtin_amdgcn_mfma_f32_16x16x32_bf16(__builtin_bit_cast(bf16x8, a), __builtin_bit_cast(bf16x8, b), c, 0, 0, 0);
}
DEVI void bar() { asm volatile("s_waitcnt lgkmcnt(0)\n\ts_barrier" ::: "memory"); }
DEVI u32 arrive(u32 a) { u32 n; asm volatile("ds_add_rtn_u32 %0, %1, %2\n\ts_waitcnt lgkmcnt(0)" : "=v"(n) : "v"(a), "v"(1u) : "memory"); return n; }
DEVI u32 arrive_nw(u32 a) { u32 n; asm volatile("ds_add_rtn_u32 %0, %1, %2" : "=v"(n) : "v"(a), "v"(1u) : "memory"); return n; }
DEVI void drain() { asm volatile("s_waitcnt vmcnt(0)" ::: "memory"); }
template <int NT>
DEVI void glds16(const void* g, u32 lds_base) {
  u32 sv;
  if (NT) asm volatile("s_mov_b32 %0, m0\n\ts_mov_b32 m0, %2\n\ts_nop 0\n\tglobal_load_lds_dwordx4 %1, off nt\n\ts_mov_b32 m0, %0"
                       : "=&s"(sv) : "v"(g), "s"(lds_base) : "memory");
  else asm volatile("s_mov_b32 %0, m0\n\ts_mov_b32 m0, %2\n\ts_nop 0\n\tglobal_load_lds_dwordx4 %1, off\n\ts_mov_b32 m0, %0"
                    : "=&s"(sv) : "v"(g), "s"(lds_base) : "memory");
}

typedef __amdgpu_buffer_rsrc_t rsrc_t;
constexpr int AUX_NT = 2, AUX_SC1 = 16;
DEVI u32x4 ldc128(rsrc_t rs, u32 off, u32 soff = 0) { return __builtin_amdgcn_raw_buffer_load_b128(rs, off, soff, AUX_SC1); }
DEVI u32x2 ldc64(rsrc_t rs, u32 off) { return __builtin_amdgcn_raw_buffer_load_b64(rs, off, 0, AUX_SC1); }
DEVI void stc128(rsrc_t rs, u32 off, u32x4 v) { __builtin_amdgcn_raw_buffer_store_b128(v, rs, off, 0, AUX_SC1); }
DEVI void stc64(rsrc_t rs, u32 off, u32x2 v) { __builtin_amdgcn_raw_buffer_store_b64(v, rs, off, 0, AUX_SC1); }
extern "C" __device__ u64 mk_dispatch_id() __asm("llvm.amdgcn.dispatch.id");
DEVI void flag_set(char* ws, u32 off, u64 tag) { __hip_atomic_store(reinterpret_cast<u64*>(ws + off), tag, RLX_AGENT); }
DEVI bool timed_out(char* ws, u64 tag, u32& spins, int lane) {
  if (++spins > SPIN_CAP) { if (lane == 0) flag_set(ws, FL_TMO, tag); return true; }
  return (spins & 255u) == 0u && __hip_atomic_load(reinterpret_cast<u64*>(ws + FL_TMO), RLX_AGENT) == tag;
}
template <int GAP>
DEVI void flags_wait(char* ws, u32 off, int first, u64 tag, int lane) {
  u32 spins = 0;
#pragma unroll 1
  for (int ph = 0; ph < 2; ++ph) {
    const u64* p = reinterpret_cast<const u64*>(ws + off + (ph == 0 ? first : lane & 31) * 128);
    for (;;) {
      if (__all(__hip_atomic_load(p, RLX_AGENT) == tag)) break;
      if (timed_out(ws, tag, spins, lane)) return;
      __builtin_amdgcn_s_sleep(16);
    }
    if (ph == 0) __builtin_amdgcn_s_sleep(GAP);
  }
}

DEVI float transpose_reduce4(const float (&x)[4], int c) {
  const bool h8 = (c & 8) != 0;
  const float s0 = h8 ? x[0] : x[2], k0 = h8 ? x[2] : x[0];
  const float s1 = h8 ? x[1] : x[3], k1 = h8 ? x[3] : x[1];
  const float y0 = k0 + dpp<ROR8>(s0), y1 = k1 + dpp<ROR8>(s1);
  const bool h4 = (c & 4) != 0;
  const float s2 = h4 ? y0 : y1, k2 = h4 ? y1 : y0;
  float z = k2 + dpp<HMIR>(s2);
  z += dpp<XOR2>(z);
  z += dpp<XOR1>(z);
  return z;
}
DEVI float score_log2(float z) { return bf16r(bf16r(z) * SCALE) * LOG2E; }
DEVI u32 cswz(u32 row) { return ((row & 3u) << 2) | ((row >> 2) & 3u); }

#define GT0 6500
#define GK 833
#define GV 835
#define GKV 100
#define GD 100
#define GD0 3
#define GD1 11
#define HOLD 128
#define LATE0 512
#define LATE1 600
#define LATE2 1100
constexpr int NS = 9, NPF = 17;

__global__ __launch_bounds__(512, 2) void mk_main(const u16* __restrict__ ax, const u16* __restrict__ kc, const u16* __restrict__ vc,
                                                  const u16* __restrict__ wq, const u16* __restrict__ wo, const u16* __restrict__ bx,
                                                  const u16* __restrict__ ckv, const u16* __restrict__ bwq, const u16* __restrict__ wuk,
                                                  const u16* __restrict__ wuv, const u16* __restrict__ bwo, u16* __restrict__ out,
                                                  char* __restrict__ ws) {
  __shared__ __attribute__((aligned(16))) char smem[L_TOTAL];
  const u32 lds0 = (u32)(size_t)(LAS char*)smem;
  int bid = blockIdx.x;
  u64 pc0;
  asm volatile("s_getpc_b64 %0" : "=s"(pc0));
  u64 t_start = __builtin_amdgcn_s_memrealtime();
  asm volatile("" : "+s"(bid));
  const int tid = threadIdx.x, lane = tid & 63, wave = __builtin_amdgcn_readfirstlane(tid >> 6);
  const int r = lane >> 4, c = lane & 15, hh = lane >> 5, l31 = lane & 31, rg = lane >> 3, cc = lane & 7;
  const int xcd = bid & 7, slot = bid >> 3;
  const int a = (slot - 8) & 31, j = a % 6, ab = j / 3, part = j % 3, rot = (4 * j) / 3;
  const int ah = (((xcd + a / 6) & 3) | (xcd & 4)) + 8 * (a / 6);
  u32 wqo = (u32)(64 * wave + 16 * r) * 8192 + ah * 256 + 16 * c;
  u64 tag;
  {
    const u64 did = mk_dispatch_id(), dp = (u64)(size_t)__builtin_amdgcn_dispatch_ptr();
    u32 lo = (u32)did ^ 0x5bd1e995u, hi = (u32)(dp >> 6) ^ (u32)(did >> 32);
    if (lo == hi) hi ^= 0x10000u;
    tag = ((u64)hi << 32) | lo;
  }
  u32 tag32 = ((u32)tag * 0x9E3779B1u) ^ ((u32)(tag >> 32) * 0x85EBCA77u);
  if (((tag32 ^ (tag32 >> 8)) & 0x00ffffffu) == 0u) tag32 ^= 0x00010200u;
  asm volatile("" : "+v"(wqo), "+s"(t_start) : "s"(rot), "s"(part), "s"(ab), "s"(tag32), "s"(ax), "s"(kc), "s"(vc), "s"(wq), "s"(bx),
               "s"(ckv), "s"(bwq), "s"(wuk), "s"(wuv), "s"(bwo), "s"(wo), "s"(ws));
  const rsrc_t rs = __builtin_amdgcn_make_buffer_rsrc(ws, 0, WS_END, 0x00020000);
  STAMP(0);

  if (__builtin_expect(slot >= 8, 1)) {
    PSTAMP(62);
    const int b = ab, h = ah, bh = b * 32 + h;
    const int p0 = (512 * part) / 3, p1 = (512 * (part + 1)) / 3;
    const u32 blk = WS_MA + (u32)(h * 2 + b) * 1024;
    const size_t kvoff = ((size_t)bh * SK + 16 * p0) * DH;
    const rsrc_t rk = __builtin_amdgcn_make_buffer_rsrc(const_cast<u16*>(kc) + kvoff, 0, (p1 - p0) * 4096, 0x00020000);
    const rsrc_t rv = __builtin_amdgcn_make_buffer_rsrc(const_cast<u16*>(vc) + kvoff, 0, (p1 - p0) * 4096, 0x00020000);
    const u32 vo = (u32)wave * 4096 + 16 * lane, ve = 21 * 32768 + (u32)wave * 1024 + 16 * lane;
    u32x4 R[8], E[2], EV[2];
#define A_ROW(t, rsc, off) do { R[t] = __builtin_amdgcn_raw_buffer_load_b128(rsc, off, 0, AUX_NT); __builtin_amdgcn_sched_barrier(0); } while (0)
    float* red = reinterpret_cast<float*>(smem + LA_SC);
    u16* qv = reinterpret_cast<u16*>(smem + LA_Q);
    {
      const rsrc_t rw = __builtin_amdgcn_make_buffer_rsrc(const_cast<u16*>(wq), 0, 512 * 4096 * 2, 0x00020000);
      u32x4 wv[16];
      u32 xp[8];
      auto wq_i = [&](int i) -> u32x4 {
        const u32 so = (u32)(2 * (((i >> 1) + rot) & 7) + (i & 1)) * 8192;
        return i < 4 ? __builtin_amdgcn_raw_buffer_load_b128(rw, wqo, so, 0) : __builtin_amdgcn_raw_buffer_load_b128(rw, wqo, so, AUX_NT);
      };
#pragma unroll
      for (int i = 0; i < 4; ++i) wv[i] = wq_i(i);
      const u32* xq = reinterpret_cast<const u32*>(ax) + b * 256 + 32 * wave + 8 * r;
#pragma unroll
      for (int jj = 0; jj < 8; ++jj) xp[jj] = xq[(jj + rot) & 7];
      if (tid < 17) stc128(rs, tid == 16 ? blk + 768 + 16 * part : blk + part * 256 + 16 * tid, u32x4{VOID, VOID, VOID, VOID});
      if (tid < 2) reinterpret_cast<u32*>(smem + LA_ML)[24 + tid] = 0u;
      asm volatile("s_barrier" ::: "memory");
      u32x4 pf[NPF];
      const int W = a * 8 + wave;
      const int slow = (1 - xcd) & 3;
      const rsrc_t rc = __builtin_amdgcn_make_buffer_rsrc(const_cast<u16*>(ckv) + (size_t)xcd * 4 * 512 * DC, 0, 4 * 512 * DC * 2, 0x00020000);
      auto pf_c = [&](int L) {
        const int q = (L >> 6) & 3;
        const int row = (L & 32) | (((L + (L >> 3) + (L >> 6)) & 7) << 2) | ((L >> 3) & 3);
        return (u32)(q * 512 + 64 * (((L >> 8) + (L >= 512 ? 1 : 0) + 2 * q) & 7) + row) * 1024 + 16 * lane;
      };
      auto pf_i = [&](int i) -> u32x4 {
        if (i >= NS) {
          const int L = NS * 192 - 1024 + 144 * (i - NS) + W - (a / 6 > slow ? 48 : 0);
          return __builtin_amdgcn_raw_buffer_load_b128(rc, a / 6 == slow || L >= 1792 ? OOB : pf_c(L), 0, 0);
        }
        const int L = W + 192 * i, Lw = L - 512;
        if (i < 2 || i >= 8) return __builtin_amdgcn_raw_buffer_load_b128(rc, pf_c(i < 2 ? L : L - 1024), 0, 0);
        const int ln = Lw < 512 ? xcd ^ 4 : xcd;
        const u16* pw = ln == 3 && (Lw & 256) == 0 ? ax : (Lw < 512 ? bwo : wo) + (size_t)(8 * (Lw & 511) + rg) * 512 + 64 * ln + 8 * cc;
        return ld16(i > 2 || L >= 512 ? pw : ckv + (size_t)xcd * 4 * 512 * DC + pf_c(L) / 2);
      };
#pragma unroll
      for (int i = 0; i < 3; ++i) pf[i] = pf_i(i);
      __builtin_amdgcn_sched_barrier(0);
#pragma unroll
      for (int i = 4; i < 16; ++i) {
        wv[i] = wq_i(i);
        __builtin_amdgcn_sched_barrier(0);
        if ((i - 4) % 6 != 0) pf[3 + (i - 4) * 5 / 6] = pf_i(3 + (i - 4) * 5 / 6);
        __builtin_amdgcn_sched_barrier(0);
      }
      asm volatile("s_barrier" ::: "memory");
#pragma unroll
      for (int i = 13; i < NPF; ++i) pf[i] = pf_i(i);
      __builtin_amdgcn_sched_barrier(0);
#pragma unroll
      for (int t = 0; t < 2; ++t) {
        EV[t] = __builtin_amdgcn_raw_buffer_load_b128(rv, ve + t * 8192, 0, AUX_NT);
        E[t] = __builtin_amdgcn_raw_buffer_load_b128(rk, ve + t * 8192, 0, AUX_NT);
      }
      __builtin_amdgcn_sched_barrier(0);
      float a0[8];
#pragma unroll
      for (int k = 0; k < 8; ++k) a0[k] = 0.f;
#pragma unroll
      for (int jj = 0; jj < 8; ++jj)
#pragma unroll
        for (int k = 0; k < 4; ++k) {
          a0[2 * k]     = dot2(perm_lo(wv[2 * jj][k], wv[2 * jj + 1][k]), xp[jj], a0[2 * k]);
          a0[2 * k + 1] = dot2(perm_hi(wv[2 * jj][k], wv[2 * jj + 1][k]), xp[jj], a0[2 * k + 1]);
        }
      PSTAMP(44);
#pragma unroll
      for (int k = 0; k < 8; ++k) a0[k] = xrow16_sum(a0[k]);
      if (r == 0) {
        *reinterpret_cast<f32x4*>(red + wave * 128 + 8 * c)     = f32x4{a0[0], a0[1], a0[2], a0[3]};
        *reinterpret_cast<f32x4*>(red + wave * 128 + 8 * c + 4) = f32x4{a0[4], a0[5], a0[6], a0[7]};
      }
      bar();
      if (tid < 128) {
        float s = 0.f;
#pragma unroll
        for (int w = 0; w < 8; ++w) s += red[w * 128 + tid];
        qv[tid] = (u16)bf16bits(s);
      }
      bar();
#pragma unroll
      for (int t = 0; t < 4; ++t) A_ROW(t, rk, vo + t * 1024);
#pragma unroll
      for (int i = 0; i < NPF; ++i) asm volatile("" :: "v"(pf[i]));
      PSTAMP(60);
    }
    STAMP(1);
    const u32x4 q4 = ld16(qv + 8 * c);
    float* sc = reinterpret_cast<float*>(smem + LA_SC) + wave * 22 * 64 + lane;
    float* ml = reinterpret_cast<float*>(smem + LA_ML);
    float* av = reinterpret_cast<float*>(smem + LA_ACC);
    float se;
    {
      float x[4] = {0.f, 0.f, 0.f, 0.f};
#pragma unroll
      for (int t = 0; t < 2; ++t) x[t] = dot2(E[t][3], q4[3], dot2(E[t][2], q4[2], dot2(E[t][1], q4[1], dot2(E[t][0], q4[0], 0.f))));
      const float z = score_log2(transpose_reduce4(x, c));
      se = wave + 8 * (c >> 2) < 4 * (p1 - p0 - 168) ? z : -INFINITY;
    }
    float m2 = se, lsum = 1.f;
    u32 now = (u32)t_start << 4, tgt = now + GT0 * 16 / 10 + (u32)((a * 8 + wave) * 37 % 64) * 5;
    int lag = 0, hold = 0;
    auto look = [&]() {
      now = (u32)__builtin_amdgcn_s_memrealtime() << 4;
      lag = (int)(now - tgt);
      hold = HOLD * 16 / 10 - max(lag, 0) * HOLD / LATE0;
    };
    auto gate = [&](u32 step) {
      tgt += step;
      asm volatile("s_cmp_lt_i32 %0, %1\n\ts_cbranch_scc0 1f\n\ts_waitcnt vmcnt(3)\n1:\n\t"
                   "s_cmp_lt_i32 %0, %2\n\ts_cbranch_scc0 2f\n\ts_waitcnt vmcnt(4)\n2:\n\ts_waitcnt vmcnt(5)"
                   :: "s"(lag), "n"(LATE1 * 16 / 10), "n"(LATE2 * 16 / 10) : "memory", "scc");
      u32 t2 = tgt;
      if (hold > 0) {
        now = (u32)__builtin_amdgcn_s_memrealtime() << 4;
        if ((int)(now + hold - t2) > 0) t2 = now + hold;
      }
      while ((int)(now - t2) < 0) {
        now = (u32)__builtin_amdgcn_s_memrealtime() << 4;
        if ((int)(now - t2) < 0) __builtin_amdgcn_s_sleep(1);
      }
    };
#pragma unroll
    for (int t = 0; t < 4; ++t) { gate(GK * 16 / 40); A_ROW(4 + t, rk, vo + 32768 + t * 1024); }
    using I0 = std::integral_constant<int, 0>;
    using I4 = std::integral_constant<int, 4>;
    auto k_page = [&](auto half, int i, rsrc_t rn, u32 on, u32 step) {
      constexpr int o = decltype(half)::value;
      look();
      float x[4];
#pragma unroll
      for (int t = 0; t < 4; ++t) {
        float d = dot2(R[o + t][0], q4[0], 0.f);
        d = dot2(R[o + t][1], q4[1], d); d = dot2(R[o + t][2], q4[2], d); d = dot2(R[o + t][3], q4[3], d);
        asm volatile("" : "+v"(d) :: "memory");
        x[t] = d;
        gate(step);
        A_ROW(o + t, rn, on + t * 1024);
      }
      PSTAMP(i);
      const float sl = score_log2(transpose_reduce4(x, c)), mn = fmaxf(m2, sl);
      lsum = fmaf(lsum, __builtin_amdgcn_exp2f(m2 - mn), __builtin_amdgcn_exp2f(sl - mn));
      m2 = mn;
      sc[i * 64] = sl;
    };
#pragma unroll 1
    for (int i = 0; i < 20; i += 2) {
      const bool lastk = i == 18;
      k_page(I0{}, i, rk, vo + (u32)(i + 2) * 32768, GK * 16 / 40);
      k_page(I4{}, i + 1, lastk ? rv : rk, lastk ? vo : vo + (u32)(i + 3) * 32768, GK * 16 / 40);
    }
    tgt += GKV * 16 / 10;
    k_page(I0{}, 20, rv, vo + 32768, GV * 16 / 40);
    STAMP(2);
    float acc[8];
    {
      const float mw = xrow16_max(row16_max(m2));
      lsum = xrow16_sum(row16_sum(lsum * __builtin_amdgcn_exp2f(m2 - mw))) * 0.25f;
      m2 = mw;
      u32 n = 0;
      if (lane == 0) { ml[wave] = m2; ml[8 + wave] = lsum; n = arrive(lds0 + LA_ML + 96); }
      if (__builtin_amdgcn_readfirstlane(n) == 7) {
        const float mv = ml[cc], lv = ml[8 + cc];
        float M = fmaxf(mv, dpp<XOR1>(mv)); M = fmaxf(M, dpp<XOR2>(M)); M = fmaxf(M, dpp<HMIR>(M));
        const float f = __builtin_amdgcn_exp2f(mv - M);
        float L = f * lv; L += dpp<XOR1>(L); L += dpp<XOR2>(L); L += dpp<HMIR>(L);
        if (lane < 8) ml[16 + lane] = f;
        if (lane == 0) stc128(rs, blk + 768 + 16 * part, u32x4{asu(M), asu(L), asu(M) ^ asu(L) ^ tag32, tag32});
      }
      const float pe = __builtin_amdgcn_exp2f(se - m2), p0_ = dpp<BCAST + 0>(pe), p1_ = dpp<BCAST + 4>(pe);
#pragma unroll
      for (int k = 0; k < 4; ++k) {
        acc[2 * k]     = fmaf(p1_, bflo(EV[1][k]), p0_ * bflo(EV[0][k]));
        acc[2 * k + 1] = fmaf(p1_, bfhi(EV[1][k]), p0_ * bfhi(EV[0][k]));
      }
    }
    auto v_page = [&](auto half, int i, bool more) {
      constexpr int o = decltype(half)::value;
      const u32 step = i + 1 >= GD0 && i + 1 < GD1 ? (GV + GD) * 16 / 40 : GV * 16 / 40;
      if (more) look();
      const float p = __builtin_amdgcn_exp2f(sc[i * 64] - m2);
      const float pb[4] = {dpp<BCAST + 0>(p), dpp<BCAST + 4>(p), dpp<BCAST + 8>(p), dpp<BCAST + 12>(p)};
#pragma unroll
      for (int t = 0; t < 4; ++t) {
#pragma unroll
        for (int k = 0; k < 4; ++k) {
          acc[2 * k]     = fmaf(pb[t], bflo(R[o + t][k]), acc[2 * k]);
          acc[2 * k + 1] = fmaf(pb[t], bfhi(R[o + t][k]), acc[2 * k + 1]);
        }
        asm volatile("" : "+v"(acc[0]), "+v"(acc[1]), "+v"(acc[2]), "+v"(acc[3]), "+v"(acc[4]), "+v"(acc[5]), "+v"(acc[6]),
                          "+v"(acc[7]) :: "memory");
        if (more) { gate(step); A_ROW(o + t, rv, vo + (u32)(i + 2) * 32768 + t * 1024); }
      }
      PSTAMP(21 + i);
    };
#pragma unroll 1
    for (int i = 0; i < 18; i += 2) { v_page(I4{}, i, true); v_page(I0{}, i + 1, true); }
    v_page(I4{}, 18, true);
    v_page(I0{}, 19, false);
    v_page(I4{}, 20, false);
    WSTAMP(8);
    {
      float u4[4], u2[2];
#pragma unroll
      for (int k = 0; k < 4; ++k) {
        auto t = __builtin_amdgcn_permlane32_swap(asu(acc[k]), asu(acc[4 + k]), false, false);
        u4[k] = asf(t[0]) + asf(t[1]);
      }
#pragma unroll
      for (int k = 0; k < 2; ++k) {
        auto t = __builtin_amdgcn_permlane16_swap(asu(u4[k]), asu(u4[2 + k]), false, false);
        u2[k] = asf(t[0]) + asf(t[1]);
      }
      *reinterpret_cast<f32x2*>(av + wave * 128 + 8 * c + 2 * r) = f32x2{u2[0], u2[1]};
    }
    {
      u32 n = 0;
      if (lane == 0) n = arrive_nw(lds0 + LA_ML + 100);
      f32x2 t[8];
#pragma unroll
      for (int w = 0; w < 8; ++w) t[w] = *reinterpret_cast<const f32x2*>(av + w * 128 + 2 * lane);
      const f32x4 f0 = *reinterpret_cast<const f32x4*>(ml + 16), f1 = *reinterpret_cast<const f32x4*>(ml + 20);
      const f32x2 o = ((t[0] * f0[0] + t[1] * f0[1]) + (t[2] * f0[2] + t[3] * f0[3])) + ((t[4] * f1[0] + t[5] * f1[1]) + (t[6] * f1[2] + t[7] * f1[3]));
      u32 e = min(pack2(o[0], o[1]), VOID - 1);
      asm volatile("" : "+v"(e), "+v"(n));
      if (__builtin_amdgcn_readfirstlane(n) == 7) {
        const u32x4 d = {dppu<0x00>(e), dppu<0x55>(e), dppu<0xAA>(e), dppu<0xFF>(e)};
        stc128(rs, (lane & 3) == 0 ? blk + part * 256 + 4 * lane : OOB, d);
        LSTAMP(3);
      }
    }
#undef A_ROW
    PDUMP();
    return;
  }

  const int col = xcd * 4 + (slot & 3), bsel = slot >> 2;
  float* red = reinterpret_cast<float*>(smem + LH_RED);
  auto slice_load = [&](const u16* m, int cg) {
    const u16* sp = m + (size_t)(512 * wave + (lane >> 1)) * 512 + 16 * cg + 8 * (lane & 1);
#pragma unroll
    for (int i = 0; i < 16; ++i) glds16<0>(sp + (size_t)i * 32 * 512, lds0 + LH_WO + wave * 16384 + i * 1024);
  };
  u32x4 wf[4][4];
  auto slice_frags = [&]() {
    LAS char* img = (LAS char*)(size_t)(lds0 + LH_WO + wave * 16384);
    const u32 q_ = (lane & 15) >> 2, pp = lane & 3;
#pragma unroll
    for (int hd = 0; hd < 4; ++hd)
#pragma unroll
      for (int ks = 0; ks < 4; ++ks) {
        s16x4 t[2];
#pragma unroll
        for (int tt = 0; tt < 2; ++tt)
          t[tt] = __builtin_amdgcn_ds_read_tr16_b64_v4i16((LAS s16x4*)(img + (128 * hd + 32 * ks + 8 * r + 4 * tt + q_) * 32 + 8 * pp));
        wf[hd][ks] = __builtin_bit_cast(u32x4, s16x8{t[0][0], t[0][1], t[0][2], t[0][3], t[1][0], t[1][1], t[1][2], t[1][3]});
      }
  };
  if (slot >= 4) {
    const int chunk = xcd * 4 + slot - 4, b = chunk >> 4;
    const u16* csrc = ckv + (size_t)chunk * 512 * DC;
    const rsrc_t rc = __builtin_amdgcn_make_buffer_rsrc(const_cast<u16*>(csrc), 0, 512 * DC * 2, 0x00020000);
    const int t0_ = 2 * (chunk & 3);
    u32x4 stg[8];
    {
      u32x4 wv[16], xs[2][2];
#pragma unroll
      for (int i = 0; i < 4; ++i) xs[i >> 1][i & 1] = ld16(bx + (i >> 1) * DM + 64 * wave + 16 * r + 8 * (i & 1));
      if (tid < 2) stc64(rs, tid == 0 ? FL_B + (u32)chunk * 128 : WS_QB + (u32)chunk * 1024, u32x2{0u, 0u});
      asm volatile("" ::: "memory");
      const u16* wp = bwq + (size_t)(64 * wave + 16 * r) * 4096 + chunk * DH + 8 * c;
#pragma unroll
      for (int i = 0; i < 16; ++i) wv[i] = ld_nt(wp + (size_t)i * 4096);
      asm volatile("s_barrier" ::: "memory");
#pragma unroll
      for (int i = 0; i < 8; ++i) {
        const u32 row = 8 * wave + i;
        stg[i] = __builtin_amdgcn_raw_buffer_load_b128(rc, ((u32)lane ^ cswz(row & 15)) << 4, (64 * ((2 + t0_) & 7) + row) * 1024, AUX_NT);
      }
      float a0[8], a1[8];
#pragma unroll
      for (int k = 0; k < 8; ++k) { a0[k] = 0.f; a1[k] = 0.f; }
#pragma unroll
      for (int jj = 0; jj < 8; ++jj) {
        const u32 x0 = xs[0][jj >> 2][jj & 3], x1 = xs[1][jj >> 2][jj & 3];
#pragma unroll
        for (int k = 0; k < 4; ++k) {
          const u32 lo = perm_lo(wv[2 * jj][k], wv[2 * jj + 1][k]), hi = perm_hi(wv[2 * jj][k], wv[2 * jj + 1][k]);
          a0[2 * k] = dot2(lo, x0, a0[2 * k]); a0[2 * k + 1] = dot2(hi, x0, a0[2 * k + 1]);
          a1[2 * k] = dot2(lo, x1, a1[2 * k]); a1[2 * k + 1] = dot2(hi, x1, a1[2 * k + 1]);
        }
      }
#pragma unroll
      for (int k = 0; k < 8; ++k) { a0[k] = xrow16_sum(a0[k]); a1[k] = xrow16_sum(a1[k]); }
      float* red = reinterpret_cast<float*>(smem + LC_SP);
      if (r == 0) {
        *reinterpret_cast<f32x4*>(red + wave * 256 + 8 * c)           = f32x4{a0[0], a0[1], a0[2], a0[3]};
        *reinterpret_cast<f32x4*>(red + wave * 256 + 8 * c + 4)       = f32x4{a0[4], a0[5], a0[6], a0[7]};
        *reinterpret_cast<f32x4*>(red + wave * 256 + 128 + 8 * c)     = f32x4{a1[0], a1[1], a1[2], a1[3]};
        *reinterpret_cast<f32x4*>(red + wave * 256 + 128 + 8 * c + 4) = f32x4{a1[4], a1[5], a1[6], a1[7]};
      }
      bar();
      if (wave == 0) {
        f32x4 t = *reinterpret_cast<const f32x4*>(red + 4 * lane);
#pragma unroll
        for (int w = 1; w < 8; ++w) t += *reinterpret_cast<const f32x4*>(red + w * 256 + 4 * lane);
        const u32 v0 = pack2(t[0], t[1]), v1 = pack2(t[2], t[3]);
        stc128(rs, WS_QB + (u32)chunk * 1024 + 16 * lane, u32x4{v0, v0 ^ tag32, v1, v1 ^ tag32});
      }
      STAMP(1);
    }
#pragma unroll
    for (int st = 0; st < 2; ++st)
#pragma unroll
      for (int i = 0; i < 8; ++i) {
        const u32 row = 8 * wave + i;
        glds16<1>(csrc + (size_t)(64 * ((st + t0_) & 7) + row) * DC + 8 * ((u32)lane ^ cswz(row & 15)), lds0 + LC_RING + st * 65536 + row * 1024);
      }
    if (wave == 0) flags_wait<20>(ws, FL_Q, 4 * xcd + 1, tag, lane);
    bar();
    STAMP(5);
    const int kq = wave & 3, hq = wave >> 2;
    const u32 fsw = cswz((u32)c) << 4;
    u32x4 qh[16], ql[16];
    {
      u32x4 t[8];
#pragma unroll
      for (int i = 0; i < 8; ++i) t[i] = ldc128(rs, WS_QF + (u32)(b * 64 + 8 * wave) * 1024 + 16 * lane, (u32)i * 1024);
      const u32 qa = lds0 + LC_SP + (16 * hq + c) * 1024 + 256 * r + fsw;
#pragma unroll
      for (int hl = 0; hl < 2; ++hl) {
#pragma unroll
        for (int i = 0; i < 4; ++i) {
          const u32 row = 4 * wave + i;
          *(LAS u32x4*)(size_t)(lds0 + LC_SP + row * 1024 + (((u32)lane ^ cswz(row & 15)) << 4)) = t[2 * i + hl];
        }
        if (hl == 0) drain();
        bar();
#pragma unroll
        for (int s = 0; s < 16; ++s) (hl ? ql[s] : qh[s]) = *(const LAS u32x4*)(size_t)(qa ^ (u32)(s << 4));
        bar();
      }
    }
    STAMP(18);
    float mrun = -INFINITY, mgrun = -INFINITY, lsum = 0.f;
    f32x16 acc[2];
#pragma unroll
    for (int ct = 0; ct < 2; ++ct)
#pragma unroll
      for (int v = 0; v < 16; ++v) acc[ct][v] = 0.f;
    const u32 blk = (lane >> 4) & 1, q_ = (lane & 15) >> 2, pp = lane & 3;
    const u32 a1 = (16 * kq + c) * 1024 + 256 * r + fsw;
    float* mx = reinterpret_cast<float*>(smem + LC_MX);
#pragma unroll 1
    for (int st = 0; st < 8; ++st) {
      const u32 tb = lds0 + LC_RING + (st & 1) * 65536;
      f32x4 sa = {0.f, 0.f, 0.f, 0.f};
      {
        u32x4 af[4];
#pragma unroll
        for (int s = 0; s < 4; ++s) af[s] = *(const LAS u32x4*)(size_t)((tb + a1) ^ (u32)(s << 4));
#pragma unroll
        for (int s = 0; s < 16; ++s) {
          __builtin_amdgcn_sched_barrier(0);
          sa = mfma16(af[s & 3], qh[s], sa);
          sa = mfma16(af[s & 3], ql[s], sa);
          __builtin_amdgcn_sched_barrier(0);
          if (s + 4 < 16) af[s & 3] = *(const LAS u32x4*)(size_t)((tb + a1) ^ (u32)((s + 4) << 4));
        }
      }
      float sl[4];
#pragma unroll
      for (int v = 0; v < 4; ++v) sl[v] = score_log2(sa[v]);
      const float tm = xrow16_max(fmaxf(fmaxf(sl[0], sl[1]), fmaxf(sl[2], sl[3])));
      if (r == 0) mx[(16 * hq + c) * 4 + kq] = tm;
      bar();
      const f32x4 ms = *reinterpret_cast<const f32x4*>(mx + (16 * hq + c) * 4), mg = *reinterpret_cast<const f32x4*>(mx + l31 * 4);
      const float mn = fmaxf(mrun, fmaxf(fmaxf(ms[0], ms[1]), fmaxf(ms[2], ms[3])));
      const float mgn = fmaxf(mgrun, fmaxf(fmaxf(mg[0], mg[1]), fmaxf(mg[2], mg[3])));
      float ps = 0.f;
#pragma unroll
      for (int v = 0; v < 4; ++v) { sl[v] = __builtin_amdgcn_exp2f(sl[v] - mn); ps += sl[v]; }
      lsum = fmaf(lsum, __builtin_amdgcn_exp2f(mrun - mn), ps);
      mrun = mn;
      *reinterpret_cast<u32x2*>(smem + LC_P + (kq * 64 + (r & 1) * 32 + 16 * hq + c) * 16 + (r >> 1) * 8) =
          u32x2{pack2(sl[0], sl[1]), pack2(sl[2], sl[3])};
      const float alpha = __builtin_amdgcn_exp2f(mgrun - mgn);
      mgrun = mgn;
#pragma unroll
      for (int ct = 0; ct < 2; ++ct)
#pragma unroll
        for (int v = 0; v < 16; ++v) acc[ct][v] *= alpha;
      if (st >= 1 && st < 7) {
        const u32 ob = lds0 + LC_RING + ((st + 1) & 1) * 65536 + wave * 8192 + 16 * lane;
        const u32 so = st < 6 ? (u32)(64 * ((st + 2 + t0_) & 7) + 8 * wave) * 1024 : OOB;
#pragma unroll
        for (int i = 0; i < 8; ++i) {
          *(LAS u32x4*)(size_t)(ob + i * 1024) = stg[i];
          asm volatile("" ::: "memory");
          stg[i] = __builtin_amdgcn_raw_buffer_load_b128(rc, (((u32)lane ^ cswz((8 * wave + i) & 15)) << 4) + so + i * 1024, 0, AUX_NT);
        }
      }
      bar();
#pragma unroll
      for (int ks = 0; ks < 4; ++ks) {
        const u32x4 pf = *reinterpret_cast<const u32x4*>(smem + LC_P + (ks * 64 + lane) * 16);
#pragma unroll
        for (int ct = 0; ct < 2; ++ct) {
          s16x4 t[2];
#pragma unroll
          for (int tt = 0; tt < 2; ++tt) {
            const u32 row = 4 * hh + 8 * tt + q_;
            const u32 pc = 8 * wave + 4 * ct + 2 * blk + (pp >> 1);
            t[tt] = __builtin_amdgcn_ds_read_tr16_b64_v4i16((LAS s16x4*)(size_t)(tb + (16 * ks + row) * 1024 + ((pc ^ cswz(row)) << 4) + 8 * (pp & 1)));
          }
          const s16x8 cf = {t[0][0], t[0][1], t[0][2], t[0][3], t[1][0], t[1][1], t[1][2], t[1][3]};
          acc[ct] = __builtin_amdgcn_mfma_f32_32x32x16_bf16(__builtin_bit_cast(bf16x8, cf), __builtin_bit_cast(bf16x8, pf), acc[ct], 0, 0, 0);
        }
      }
    }
    STAMP(19);
    lsum = xrow16_sum(lsum);
    float* lsb = reinterpret_cast<float*>(smem + LC_LS);
    if (r == 0) lsb[kq * 32 + 16 * hq + c] = lsum;
#pragma unroll
    for (int ct = 0; ct < 2; ++ct)
#pragma unroll
      for (int v4 = 0; v4 < 4; ++v4)
        *reinterpret_cast<u32x2*>(smem + LC_RING + l31 * 1040 + (64 * wave + 32 * ct + 8 * v4 + 4 * hh) * 2) =
            u32x2{pack2(acc[ct][4 * v4], acc[ct][4 * v4 + 1]), pack2(acc[ct][4 * v4 + 2], acc[ct][4 * v4 + 3])};
    bar();
#pragma unroll
    for (int i = 0; i < 4; ++i) {
      const int hd = 8 * i + wave;
      __builtin_amdgcn_raw_buffer_store_b128(*reinterpret_cast<const u32x4*>(smem + LC_RING + hd * 1040 + 16 * lane), rs,
                                             WS_LATP + (u32)(chunk * 32 + hd) * 1024 + 16 * lane, 0, AUX_SC1 | AUX_NT);
    }
    if (tid < 32) stc64(rs, WS_LML + (u32)(chunk * 32 + tid) * 8,
                        u32x2{asu(mgrun), asu((lsb[tid] + lsb[32 + tid]) + (lsb[64 + tid] + lsb[96 + tid]))});
    if (tid < 2) reinterpret_cast<u32*>(smem + LH_W + 128)[tid] = 0u;
    drain();
    bar();
    if (tid == 0) flag_set(ws, FL_B + (u32)chunk * 128, tag);
    STAMP(3);
    slice_load(bwo, col ^ 16);
    drain();
    slice_frags();
#pragma unroll
    for (int hd = 0; hd < 4; ++hd) asm volatile("" : "+v"(wf[hd][0]), "+v"(wf[hd][1]), "+v"(wf[hd][2]), "+v"(wf[hd][3]) :: "memory");
    slice_load(wo, col);
    drain();
    STAMP(23);
  } else {

  const int h = xcd * 4 + slot;
  u32x4 wv[16];
  {
    u16* qv = reinterpret_cast<u16*>(smem + LH_Q);
    u16* qs = reinterpret_cast<u16*>(smem + LH_QS);
    u32x4 wk[16];
    {
      const u16* kp = wuk + (size_t)(64 * wave + r) * 4096 + h * DH + 8 * c;
      const u16* vp = wuv + (size_t)(64 * wave + r) * 4096 + h * DH + 8 * c;
#pragma unroll
      for (int i = 0; i < 16; ++i) wk[i] = ld_nt(kp + (size_t)i * 4 * 4096);
      if (tid < 33) stc128(rs, tid == 32 ? FL_Q + (u32)h * 128 : WS_MA + (u32)(64 + h * 2 + r) * 1024 + 16 * c,
                           u32x4{VOID, VOID, VOID, VOID});
      if (tid < 2) reinterpret_cast<u32*>(smem + LH_W + 128)[tid] = 0u;
      asm volatile("" ::: "memory");
#pragma unroll
      for (int i = 0; i < 16; ++i) wv[i] = ld_nt(vp + (size_t)i * 4 * 4096);
      asm volatile("" ::: "memory");
    }
    if (wave == 0) {
      u32 spins = 0;
      u32x4 g;
      for (;;) {
        g = ldc128(rs, WS_QB + (u32)h * 1024 + 16 * lane);
        if (__all(((g[0] ^ g[1] ^ tag32) | (g[2] ^ g[3] ^ tag32)) == 0u) || timed_out(ws, tag, spins, lane)) break;
        __builtin_amdgcn_s_sleep(4);
      }
      *reinterpret_cast<u32x2*>(qv + 4 * lane) = u32x2{g[0], g[2]};
    }
    bar();
#pragma unroll
    for (int bb = 0; bb < 2; ++bb) {
      const u32x4 q4 = ld16(qv + bb * 128 + 8 * c);
#pragma unroll
      for (int g = 0; g < 4; ++g) {
        float x[4];
#pragma unroll
        for (int t = 0; t < 4; ++t) {
          const u32x4 w4 = wk[4 * g + t];
          float d = dot2(w4[0], q4[0], 0.f);
          d = dot2(w4[1], q4[1], d); d = dot2(w4[2], q4[2], d); d = dot2(w4[3], q4[3], d);
          x[t] = d;
        }
        const float z = transpose_reduce4(x, c);
        if ((c & 3) == 0) {
          const int ci = 64 * wave + 16 * g + 4 * (c >> 2) + r;
          const u32 hi = bf16bits(z);
          qs[(bb * 2 + 0) * 512 + ci] = (u16)hi;
          qs[(bb * 2 + 1) * 512 + ci] = (u16)bf16bits(z - asf(hi << 16));
        }
      }
    }
    bar();
    if (tid < 256)
      stc128(rs, WS_QF + (u32)(((wave >> 1) * 32 + h) * 2 + (wave & 1)) * 1024 + 16 * lane, *reinterpret_cast<const u32x4*>(qs + 8 * tid));
    drain();
    bar();
    if (tid == 0) flag_set(ws, FL_Q + (u32)h * 128, tag);
    STAMP(1);
  }
  {
    slice_load(bwo, col ^ 16);
    drain();
    slice_frags();
#pragma unroll
    for (int hd = 0; hd < 4; ++hd) asm volatile("" : "+v"(wf[hd][0]), "+v"(wf[hd][1]), "+v"(wf[hd][2]), "+v"(wf[hd][3]) :: "memory");
    slice_load(wo, col);
    STAMP(17);
    {
      const rsrc_t rcode = __builtin_amdgcn_make_buffer_rsrc((void*)(pc0 & ~255ull), 0, 32768, 0x00020000);
      const u32x4 c0 = __builtin_amdgcn_raw_buffer_load_b128(rcode, (u32)(slot * 8 + wave) * 1024 + 16 * lane, 0, 0);
      asm volatile("" :: "v"(c0));
    }
    if (wave == 0) flags_wait<40>(ws, FL_B, h, tag, lane);
    bar();
    STAMP(2);
    float* cw = reinterpret_cast<float*>(smem + LH_W);
    float* part_ = reinterpret_cast<float*>(smem + LH_PART);
    float* lat = reinterpret_cast<float*>(smem + LH_LAT);
    u16* ctx = reinterpret_cast<u16*>(smem + LH_CTX);
    {
      const int bb = wave >> 2, qt = wave & 3;
      const u32 lo = WS_LATP + (u32)(4 * wave * 32 + h) * 1024 + 16 * lane;
      u32x4 v[4];
#pragma unroll
      for (int i = 0; i < 4; ++i) v[i] = __builtin_amdgcn_raw_buffer_load_b128(rs, lo, (u32)i * 32 * 1024, AUX_SC1 | AUX_NT);
      if (wave == 0) {
        const u32x2 ml = ldc64(rs, WS_LML + (u32)(l31 * 32 + h) * 8);
        const float mj = asf(ml[0]), lj = asf(ml[1]);
        const float M = row16_max(mj);
        const float w = __builtin_amdgcn_exp2f(mj - M);
        const float L = row16_sum(w * lj);
        cw[l31] = w / L;
      }
      bar();
      float a8[8];
#pragma unroll
      for (int e = 0; e < 8; ++e) a8[e] = 0.f;
#pragma unroll
      for (int i = 0; i < 4; ++i) {
        const float w = cw[4 * wave + i];
#pragma unroll
        for (int e = 0; e < 4; ++e) { a8[2 * e] = fmaf(w, bflo(v[i][e]), a8[2 * e]); a8[2 * e + 1] = fmaf(w, bfhi(v[i][e]), a8[2 * e + 1]); }
      }
      *reinterpret_cast<f32x4*>(part_ + (qt * 2 + bb) * 512 + 8 * lane)     = f32x4{a8[0], a8[1], a8[2], a8[3]};
      *reinterpret_cast<f32x4*>(part_ + (qt * 2 + bb) * 512 + 8 * lane + 4) = f32x4{a8[4], a8[5], a8[6], a8[7]};
      bar();
      lat[tid] = (part_[tid] + part_[1024 + tid]) + (part_[2048 + tid] + part_[3072 + tid]);
      lat[512 + tid] = (part_[512 + tid] + part_[1536 + tid]) + (part_[2560 + tid] + part_[3584 + tid]);
      bar();
    }
    {
      float a0[8], a1[8];
#pragma unroll
      for (int k = 0; k < 8; ++k) { a0[k] = 0.f; a1[k] = 0.f; }
#pragma unroll
      for (int i = 0; i < 16; ++i) {
        const float l0 = lat[64 * wave + 4 * i + r], l1 = lat[512 + 64 * wave + 4 * i + r];
#pragma unroll
        for (int k = 0; k < 4; ++k) {
          a0[2 * k] = fmaf(l0, bflo(wv[i][k]), a0[2 * k]); a0[2 * k + 1] = fmaf(l0, bfhi(wv[i][k]), a0[2 * k + 1]);
          a1[2 * k] = fmaf(l1, bflo(wv[i][k]), a1[2 * k]); a1[2 * k + 1] = fmaf(l1, bfhi(wv[i][k]), a1[2 * k + 1]);
        }
      }
#pragma unroll
      for (int k = 0; k < 8; ++k) { a0[k] = xrow16_sum(a0[k]); a1[k] = xrow16_sum(a1[k]); }
      if (r == 0) {
        *reinterpret_cast<f32x4*>(red + wave * 256 + 8 * c)           = f32x4{a0[0], a0[1], a0[2], a0[3]};
        *reinterpret_cast<f32x4*>(red + wave * 256 + 8 * c + 4)       = f32x4{a0[4], a0[5], a0[6], a0[7]};
        *reinterpret_cast<f32x4*>(red + wave * 256 + 128 + 8 * c)     = f32x4{a1[0], a1[1], a1[2], a1[3]};
        *reinterpret_cast<f32x4*>(red + wave * 256 + 128 + 8 * c + 4) = f32x4{a1[4], a1[5], a1[6], a1[7]};
      }
      bar();
      if (tid < 256) {
        float s = 0.f;
#pragma unroll
        for (int w = 0; w < 8; ++w) s += red[w * 256 + tid];
        ctx[tid] = (u16)bf16bits(s);
      }
      bar();
      if (tid < 32) {
        u32x4 d = ld16(ctx + 8 * tid);
#pragma unroll
        for (int k = 0; k < 4; ++k) d[k] = min(d[k], VOID - 1);
        stc128(rs, WS_MA + (u32)(64 + h * 2 + r) * 1024 + 16 * c, d);
      }
    }
    STAMP(4);
  }
  }
#pragma unroll 1
  for (int pa = 0; pa < 2; ++pa) {
    const bool skip = (c & 3) >= (pa ? 3 : 1);
    const u32 b0 = WS_MA + (u32)((pa ? 0 : 64) + 8 * wave + ((c >> 1) & 2) + bsel) * 1024;
    u32 spins = 0;
    float wt[2] = {1.f, 1.f};
    if (pa) {
      slice_frags();
      u32x4 hd[2];
      for (;;) {
        u32 bad = 0;
#pragma unroll
        for (int k = 0; k < 2; ++k) hd[k] = ldc128(rs, skip ? OOB : b0 + 768 + 16 * (c & 3), (u32)k * 4096);
#pragma unroll
        for (int k = 0; k < 2; ++k) bad |= (hd[k][3] ^ tag32) | (hd[k][0] ^ hd[k][1] ^ hd[k][2] ^ tag32);
        if (__all(skip || bad == 0u) || timed_out(ws, tag, spins, lane)) break;
        __builtin_amdgcn_s_sleep(16);
      }
#pragma unroll
      for (int k = 0; k < 2; ++k) {
        const float mm = skip ? -INFINITY : asf(hd[k][0]);
        float M = fmaxf(mm, dpp<XOR1>(mm)); M = fmaxf(M, dpp<XOR2>(M));
        const float ee = __builtin_amdgcn_exp2f(mm - M);
        float L = ee * asf(hd[k][1]); L += dpp<XOR1>(L); L += dpp<XOR2>(L);
        wt[k] = ee * __builtin_amdgcn_rcpf(L);
      }
    } else __builtin_amdgcn_s_sleep(32);
    const u32 od = b0 + (c & 3) * 256 + (c >> 3) * 64 + 16 * r;
    STAMP(20 + pa);
    u32x4 d[2][2];
    f32x4 e[2];
    bool miss[2];
#pragma unroll
    for (int k = 0; k < 2; ++k) { d[k][0] = d[k][1] = u32x4{0u, 0u, 0u, 0u}; e[k] = f32x4{0.f, 0.f, 0.f, 0.f}; miss[k] = !skip; }
    u32 pend = 3;
    for (;;) {
#pragma unroll
      for (int k = 0; k < 2; ++k)
        if (miss[k]) { d[k][0] = ldc128(rs, od, (u32)k * 4096); d[k][1] = ldc128(rs, od + 128, (u32)k * 4096); }
#pragma unroll
      for (int k = 0; k < 2; ++k) {
        if (!((pend >> k) & 1)) continue;
        miss[k] = max(max(max(d[k][0][0], d[k][0][1]), max(d[k][0][2], d[k][0][3])), max(max(d[k][1][0], d[k][1][1]), max(d[k][1][2], d[k][1][3]))) == VOID;
        if (__any(miss[k])) continue;
        pend &= ~(1u << k);
        const f32x4 z = {0.f, 0.f, 0.f, 0.f};
        f32x4 t[4];
#pragma unroll
        for (int q = 0; q < 4; ++q) t[q] = mfma16(wf[2 * k + (q >> 1)][q & 1], d[k][0], z);
#pragma unroll
        for (int q = 0; q < 4; ++q) t[q] = mfma16(wf[2 * k + (q >> 1)][2 + (q & 1)], d[k][1], t[q]);
#pragma unroll
        for (int v = 0; v < 4; ++v) e[k][v] = wt[k] * ((c & 4) ? ((c & 8) ? t[3][v] : t[2][v]) : ((c & 8) ? t[1][v] : t[0][v]));
      }
      if (pend == 0 || timed_out(ws, tag, spins, lane)) break;
      if (pa) __builtin_amdgcn_s_sleep(1); else __builtin_amdgcn_s_sleep(4);
    }
    WSTAMP(8);
    f32x4 y = e[0] + e[1];
#pragma unroll
    for (int v = 0; v < 4; ++v) y[v] = row16_sum(y[v]);
    float* fin = red + 128 * pa;
    if (c == 0) *reinterpret_cast<f32x4*>(fin + wave * 16 + 4 * r) = y;
    u32 n = 0;
    if (lane == 0) n = arrive_nw(lds0 + LH_W + 128 + 4 * pa);
    float t[8];
#pragma unroll
    for (int w = 0; w < 8; ++w) t[w] = fin[w * 16 + c];
    u32 o = bf16bits(((t[0] + t[1]) + (t[2] + t[3])) + ((t[4] + t[5]) + (t[6] + t[7])));
    asm volatile("" : "+v"(o), "+v"(n));
    if (__builtin_amdgcn_readfirstlane(n) == 7) {
      if (lane < 16) out[bsel * 1024 + (pa ? 16 * col : 512 + 16 * (col ^ 16)) + c] = (u16)o;
      LSTAMP(7);
    }
  }
}

extern "C" void kernel_launch(void* const* d_in, const int* in_sizes, int n_in, void* d_out, int out_size, void* d_ws, size_t ws_size,
                              hipStream_t stream) {
  mk_main<<<256, 512, 0, stream>>>((const u16*)d_in[0], (const u16*)d_in[1], (const u16*)d_in[2], (const u16*)d_in[3],
                                   (const u16*)d_in[4], (const u16*)d_in[5], (const u16*)d_in[6], (const u16*)d_in[7],
                                   (const u16*)d_in[8], (const u16*)d_in[9], (const u16*)d_in[10], (u16*)d_out, (char*)d_ws);
}
```

```cpp
#include <hip/hip_runtime.h>
#include <hip/hip_bf16.h>
#include <stdint.h>
#include <type_traits>

typedef unsigned short u16;
typedef unsigned int u32;
typedef unsigned long long u64;
using u32x2  = __attribute__((ext_vector_type(2))) u32;
using u32x4  = __attribute__((ext_vector_type(4))) u32;
using f32x2  = __attribute__((ext_vector_type(2))) float;
using f32x4  = __attribute__((ext_vector_type(4))) float;
using f32x16 = __attribute__((ext_vector_type(16))) float;
using s16x4  = __attribute__((ext_vector_type(4))) short;
using s16x8  = __attribute__((ext_vector_type(8))) short;
using bf16x2 = __attribute__((ext_vector_type(2))) __bf16;
using bf16x8 = __attribute__((ext_vector_type(8))) __bf16;
#define LAS __attribute__((address_space(3)))
#define DEVI __device__ __forceinline__
#define RLX_AGENT __ATOMIC_RELAXED, __HIP_MEMORY_SCOPE_AGENT

constexpr float SCALE = 0.088388347648318440f;
constexpr float LOG2E = 1.4426950408889634f;
constexpr int SK = 8192, DH = 128, DM = 512, DC = 512;

constexpr u32 FL_Q = 0, FL_B = 4096, FL_TMO = 8192;
constexpr u32 WS_MA   = 12288;
constexpr u32 VOID = 0xffffffffu;
constexpr u32 WS_QB   = WS_MA + 128 * 1024;
constexpr u32 WS_QF   = WS_QB + 32 * 1024;
constexpr u32 WS_LML  = WS_QF + 2 * 32 * 2 * 512 * 2;
constexpr u32 WS_LATP = WS_LML + 32 * 32 * 2 * 4;
constexpr u32 WS_END  = WS_LATP + 32 * 32 * 512 * 2;
constexpr u32 OOB = 0x80000000u;
constexpr u32 SPIN_CAP = 1u << 17;
#define PSTAMP(i) do { } while (0)
#define PDUMP() do { } while (0)
#define STAMP(i) do { } while (0)
#define LSTAMP(i) do { } while (0)
#define WSTAMP(i) do { } while (0)

constexpr int L_TOTAL = 163840;
constexpr int LA_Q   = 0;
constexpr int LA_ML  = 256;
constexpr int LA_ACC = 512;
constexpr int LA_SC  = 8192;
constexpr int LC_RING = 0;
constexpr int LC_SP   = 131072;
constexpr int LC_MX   = LC_SP;
constexpr int LC_P    = LC_SP + 512;
constexpr int LC_LS   = LC_P + 4096;
constexpr int LH_WO   = 0;
constexpr int LH_RED  = 131072;
constexpr int LH_W    = LH_RED + 8192;
constexpr int LH_CTX  = LH_W + 256;
constexpr int LH_LAT  = LH_CTX + 512;
constexpr int LH_PART = LH_LAT + 4096;
constexpr int LH_Q    = LH_PART;
constexpr int LH_QS   = LH_Q + 512;
static_assert(LH_PART + 16384 <= L_TOTAL, "LDS");

DEVI float dot2(u32 a, u32 b, float c) {
  return __builtin_amdgcn_fdot2_f32_bf16(__builtin_bit_cast(bf16x2, a), __builtin_bit_cast(bf16x2, b), c, false);
}
DEVI u32 bf16bits(float x) { return (u32)__builtin_bit_cast(u16, (__bf16)x); }
DEVI float bf16r(float x) { return __builtin_bit_cast(float, bf16bits(x) << 16); }
DEVI float bflo(u32 v) { return __builtin_bit_cast(float, v << 16); }
DEVI float bfhi(u32 v) { return __builtin_bit_cast(float, v & 0xffff0000u); }
DEVI float asf(u32 v) { return __builtin_bit_cast(float, v); }
DEVI u32 asu(float v) { return __builtin_bit_cast(u32, v); }
DEVI u32 pack2(float lo, float hi) { return __builtin_bit_cast(u32, __builtin_convertvector(f32x2{lo, hi}, bf16x2)); }
template <int CTRL> DEVI float dpp(float x) {
  return __builtin_bit_cast(float, __builtin_amdgcn_mov_dpp(__builtin_bit_cast(int, x), CTRL, 0xf, 0xf, true));
}
template <int CTRL> DEVI u32 dppu(u32 x) { return (u32)__builtin_amdgcn_mov_dpp((int)x, CTRL, 0xf, 0xf, true); }
constexpr int XOR1 = 0xB1, XOR2 = 0x4E, HMIR = 0x141, ROR8 = 0x128, ROR4 = 0x124, BCAST = 0x150;
DEVI float xrow16_sum(float x) {
  auto s = __builtin_amdgcn_permlane16_swap(__float_as_uint(x), __float_as_uint(x), false, false);
  x = __uint_as_float(s[0]) + __uint_as_float(s[1]);
  auto t = __builtin_amdgcn_permlane32_swap(__float_as_uint(x), __float_as_uint(x), false, false);
  return __uint_as_float(t[0]) + __uint_as_float(t[1]);
}
DEVI float xrow16_max(float x) {
  auto s = __builtin_amdgcn_permlane16_swap(__float_as_uint(x), __float_as_uint(x), false, false);
  x = fmaxf(__uint_as_float(s[0]), __uint_as_float(s[1]));
  auto t = __builtin_amdgcn_permlane32_swap(__float_as_uint(x), __float_as_uint(x), false, false);
  return fmaxf(__uint_as_float(t[0]), __uint_as_float(t[1]));
}
DEVI float row16_sum(float x) { x += dpp<XOR1>(x); x += dpp<XOR2>(x); x += dpp<ROR4>(x); x += dpp<ROR8>(x); return x; }
DEVI float row16_max(float x) { x = fmaxf(x, dpp<XOR1>(x)); x = fmaxf(x, dpp<XOR2>(x)); x = fmaxf(x, dpp<ROR4>(x)); x = fmaxf(x, dpp<ROR8>(x)); return x; }
DEVI u32x4 ld_nt(const u16* p) { return __builtin_nontemporal_load(reinterpret_cast<const u32x4*>(p)); }
DEVI u32x4 ld16(const u16* p) { return *reinterpret_cast<const u32x4*>(p); }
DEVI u32 perm_lo(u32 a, u32 b) { return __builtin_amdgcn_perm(b, a, 0x05040100u); }
DEVI u32 perm_hi(u32 a, u32 b) { return __builtin_amdgcn_perm(b, a, 0x07060302u); }
DEVI f32x4 mfma16(const u32x4& a, const u32x4& b, const f32x4& c) {
  return __builtin_amdgcn_mfma_f32_16x16x32_bf16(__builtin_bit_cast(bf16x8, a), __builtin_bit_cast(bf16x8, b), c, 0, 0, 0);
}
DEVI void bar() { asm volatile("s_waitcnt lgkmcnt(0)\n\ts_barrier" ::: "memory"); }
DEVI u32 arrive(u32 a) { u32 n; asm volatile("ds_add_rtn_u32 %0, %1, %2\n\ts_waitcnt lgkmcnt(0)" : "=v"(n) : "v"(a), "v"(1u) : "memory"); return n; }
DEVI u32 arrive_nw(u32 a) { u32 n; asm volatile("ds_add_rtn_u32 %0, %1, %2" : "=v"(n) : "v"(a), "v"(1u) : "memory"); return n; }
DEVI void drain() { asm volatile("s_waitcnt vmcnt(0)" ::: "memory"); }
template <int NT>
DEVI void glds16(const void* g, u32 lds_base) {
  u32 sv;
  if (NT) asm volatile("s_mov_b32 %0, m0\n\ts_mov_b32 m0, %2\n\ts_nop 0\n\tglobal_load_lds_dwordx4 %1, off nt\n\ts_mov_b32 m0, %0"
                       : "=&s"(sv) : "v"(g), "s"(lds_base) : "memory");
  else asm volatile("s_mov_b32 %0, m0\n\ts_mov_b32 m0, %2\n\ts_nop 0\n\tglobal_load_lds_dwordx4 %1, off\n\ts_mov_b32 m0, %0"
                    : "=&s"(sv) : "v"(g), "s"(lds_base) : "memory");
}

typedef __amdgpu_buffer_rsrc_t rsrc_t;
constexpr int AUX_NT = 2, AUX_SC1 = 16;
DEVI u32x4 ldc128(rsrc_t rs, u32 off, u32 soff = 0) { return __builtin_amdgcn_raw_buffer_load_b128(rs, off, soff, AUX_SC1); }
DEVI u32x2 ldc64(rsrc_t rs, u32 off) { return __builtin_amdgcn_raw_buffer_load_b64(rs, off, 0, AUX_SC1); }
DEVI void stc128(rsrc_t rs, u32 off, u32x4 v) { __builtin_amdgcn_raw_buffer_store_b128(v, rs, off, 0, AUX_SC1); }
DEVI void stc64(rsrc_t rs, u32 off, u32x2 v) { __builtin_amdgcn_raw_buffer_store_b64(v, rs, off, 0, AUX_SC1); }
extern "C" __device__ u64 mk_dispatch_id() __asm("llvm.amdgcn.dispatch.id");
DEVI void flag_set(char* ws, u32 off, u64 tag) { __hip_atomic_store(reinterpret_cast<u64*>(ws + off), tag, RLX_AGENT); }
DEVI bool timed_out(char* ws, u64 tag, u32& spins, int lane) {
  if (++spins > SPIN_CAP) { if (lane == 0) flag_set(ws, FL_TMO, tag); return true; }
  return (spins & 255u) == 0u && __hip_atomic_load(reinterpret_cast<u64*>(ws + FL_TMO), RLX_AGENT) == tag;
}
template <int GAP>
DEVI void flags_wait(char* ws, u32 off, int first, u64 tag, int lane) {
  u32 spins = 0;
#pragma unroll 1
  for (int ph = 0; ph < 2; ++ph) {
    const u64* p = reinterpret_cast<const u64*>(ws + off + (ph == 0 ? first : lane & 31) * 128);
    for (;;) {
      if (__all(__hip_atomic_load(p, RLX_AGENT) == tag)) break;
      if (timed_out(ws, tag, spins, lane)) return;
      __builtin_amdgcn_s_sleep(16);
    }
    if (ph == 0) __builtin_amdgcn_s_sleep(GAP);
  }
}

DEVI float transpose_reduce4(const float (&x)[4], int c) {
  const bool h8 = (c & 8) != 0;
  const float s0 = h8 ? x[0] : x[2], k0 = h8 ? x[2] : x[0];
  const float s1 = h8 ? x[1] : x[3], k1 = h8 ? x[3] : x[1];
  const float y0 = k0 + dpp<ROR8>(s0), y1 = k1 + dpp<ROR8>(s1);
  const bool h4 = (c & 4) != 0;
  const float s2 = h4 ? y0 : y1, k2 = h4 ? y1 : y0;
  float z = k2 + dpp<HMIR>(s2);
  z += dpp<XOR2>(z);
  z += dpp<XOR1>(z);
  return z;
}
DEVI float score_log2(float z) { return bf16r(bf16r(z) * SCALE) * LOG2E; }
DEVI u32 cswz(u32 row) { return ((row & 3u) << 2) | ((row >> 2) & 3u); }

#define GT0 6500
#define GK 830
#define GV 800
#define GKV 100
#define GD 100
#define GD0 3
#define GD1 11
#define HOLD 128
#define LATE0 512
#define LATE1 600
#define LATE2 1100
#define SYN 1
#define RUP 200
#define RDN 600
#define LPP 4
constexpr int NS = 9, NPF = 17;

__global__ __launch_bounds__(512, 2) void mk_main(const u16* __restrict__ ax, const u16* __restrict__ kc, const u16* __restrict__ vc,
                                                  const u16* __restrict__ wq, const u16* __restrict__ wo, const u16* __restrict__ bx,
                                                  const u16* __restrict__ ckv, const u16* __restrict__ bwq, const u16* __restrict__ wuk,
                                                  const u16* __restrict__ wuv, const u16* __restrict__ bwo, u16* __restrict__ out,
                                                  char* __restrict__ ws) {
  __shared__ __attribute__((aligned(16))) char smem[L_TOTAL];
  const u32 lds0 = (u32)(size_t)(LAS char*)smem;
  int bid = blockIdx.x;
  u64 pc0;
  asm volatile("s_getpc_b64 %0" : "=s"(pc0));
  u64 t_start = __builtin_amdgcn_s_memrealtime();
  asm volatile("" : "+s"(bid));
  const int tid = threadIdx.x, lane = tid & 63, wave = __builtin_amdgcn_readfirstlane(tid >> 6);
  const int r = lane >> 4, c = lane & 15, hh = lane >> 5, l31 = lane & 31, rg = lane >> 3, cc = lane & 7;
  const int xcd = bid & 7, slot = bid >> 3;
  const int a = (slot - 8) & 31, j = a % 6, ab = j / 3, part = j % 3, rot = (4 * j) / 3;
  const int ah = (((xcd + a / 6) & 3) | (xcd & 4)) + 8 * (a / 6);
  u32 wqo = (u32)(64 * wave + 16 * r) * 8192 + ah * 256 + 16 * c;
  u64 tag;
  {
    const u64 did = mk_dispatch_id(), dp = (u64)(size_t)__builtin_amdgcn_dispatch_ptr();
    u32 lo = (u32)did ^ 0x5bd1e995u, hi = (u32)(dp >> 6) ^ (u32)(did >> 32);
    if (lo == hi) hi ^= 0x10000u;
    tag = ((u64)hi << 32) | lo;
  }
  u32 tag32 = ((u32)tag * 0x9E3779B1u) ^ ((u32)(tag >> 32) * 0x85EBCA77u);
  if (((tag32 ^ (tag32 >> 8)) & 0x00ffffffu) == 0u) tag32 ^= 0x00010200u;
  asm volatile("" : "+v"(wqo), "+s"(t_start) : "s"(rot), "s"(part), "s"(ab), "s"(tag32), "s"(ax), "s"(kc), "s"(vc), "s"(wq), "s"(bx),
               "s"(ckv), "s"(bwq), "s"(wuk), "s"(wuv), "s"(bwo), "s"(wo), "s"(ws));
  const rsrc_t rs = __builtin_amdgcn_make_buffer_rsrc(ws, 0, WS_END, 0x00020000);
  STAMP(0);

  if (__builtin_expect(slot >= 8, 1)) {
    PSTAMP(62);
    const int b = ab, h = ah, bh = b * 32 + h;
    const int e0 = (8 * part) / 3, e1 = (8 * (part + 1)) / 3;
    const u32 blk = WS_MA + (u32)(h * 2 + b) * 1024;
    const size_t kvoff = (size_t)bh * SK * DH;
    const rsrc_t rk = __builtin_amdgcn_make_buffer_rsrc(const_cast<u16*>(kc) + kvoff, 0, (504 + e1) * 4096, 0x00020000);
    const rsrc_t rv = __builtin_amdgcn_make_buffer_rsrc(const_cast<u16*>(vc) + kvoff, 0, (504 + e1) * 4096, 0x00020000);
    constexpr u32 WIN = 3 * 32768;
    const u32 vo = (u32)(part * 8 + wave) * 4096 + 16 * lane, ve = (u32)(504 + e0) * 4096 + (u32)wave * 1024 + 16 * lane;
    u32 ro[4];
#pragma unroll
    for (int t = 0; t < 4; ++t) ro[t] = (u32)((t + 2 * b + (h >> 2 & 1)) & 3) * 1024;
    u32x4 R[8], E[2], EV[2];
#define A_ROW(t, rsc, off) do { R[t] = __builtin_amdgcn_raw_buffer_load_b128(rsc, off, ro[(t) & 3], AUX_NT); __builtin_amdgcn_sched_barrier(0); } while (0)
    float* red = reinterpret_cast<float*>(smem + LA_SC);
    u16* qv = reinterpret_cast<u16*>(smem + LA_Q);
    {
      const rsrc_t rw = __builtin_amdgcn_make_buffer_rsrc(const_cast<u16*>(wq), 0, 512 * 4096 * 2, 0x00020000);
      u32x4 wv[16];
      u32 xp[8];
      auto wq_i = [&](int i) -> u32x4 {
        const u32 so = (u32)(2 * (((i >> 1) + rot) & 7) + (i & 1)) * 8192;
        return i < 4 ? __builtin_amdgcn_raw_buffer_load_b128(rw, wqo, so, 0) : __builtin_amdgcn_raw_buffer_load_b128(rw, wqo, so, AUX_NT);
      };
#pragma unroll
      for (int i = 0; i < 4; ++i) wv[i] = wq_i(i);
      const u32* xq = reinterpret_cast<const u32*>(ax) + b * 256 + 32 * wave + 8 * r;
#pragma unroll
      for (int jj = 0; jj < 8; ++jj) xp[jj] = xq[(jj + rot) & 7];
      if (tid < 17) stc128(rs, tid == 16 ? blk + 768 + 16 * part : blk + part * 256 + 16 * tid, u32x4{VOID, VOID, VOID, VOID});
      if (tid < 2) reinterpret_cast<u32*>(smem + LA_ML)[24 + tid] = 0u;
      asm volatile("s_barrier" ::: "memory");
      u32x4 pf[NPF];
      const int W = a * 8 + wave;
      const int slow = (1 - xcd) & 3;
      const rsrc_t rc = __builtin_amdgcn_make_buffer_rsrc(const_cast<u16*>(ckv) + (size_t)xcd * 4 * 512 * DC, 0, 4 * 512 * DC * 2, 0x00020000);
      auto pf_c = [&](int L) {
        const bool one = L >= 512 && L < 768;
        const int M = L >= 768 ? L + 256 : L, q = (one ? L >> 6 : M >> 7) & 3, tl = one ? 3 : 2 * (M >> 9) + ((M >> 6) & 1);
        const int row = (L & 32) | (((L + (L >> 3) + (L >> 6)) & 7) << 2) | ((L >> 3) & 3);
        return (u32)(q * 512 + 64 * ((tl + 2 * q + (xcd & 2)) & 7) + row) * 1024 + 16 * lane;
      };
      auto pf_i = [&](int i) -> u32x4 {
        if (i >= NS) {
          const int L = NS * 192 - 1024 + 144 * (i - NS) + W - (a / 6 > slow ? 48 : 0);
          return __builtin_amdgcn_raw_buffer_load_b128(rc, a / 6 == slow || L >= 1792 ? OOB : pf_c(L), 0, 0);
        }
        const int L = W + 192 * i, Lw = L - 512;
        if (i < 2 || i >= 8) return __builtin_amdgcn_raw_buffer_load_b128(rc, pf_c(i < 2 ? L : L - 1024), 0, 0);
        const int ln = Lw < 512 ? xcd ^ 4 : xcd;
        const int G = ((Lw & 4) << 6) | ((Lw >> 1) & 0xfc) | (Lw & 3);
        const u16* pw = ln == 3 && G < 256 ? ax : (Lw < 512 ? bwo : wo) + (size_t)(8 * G + rg) * 512 + 64 * ln + 8 * cc;
        return ld16(i > 2 || L >= 512 ? pw : ckv + (size_t)xcd * 4 * 512 * DC + pf_c(L) / 2);
      };
#pragma unroll
      for (int i = 0; i < 3; ++i) pf[i] = pf_i(i);
      __builtin_amdgcn_sched_barrier(0);
#pragma unroll
      for (int i = 4; i < 16; ++i) {
        wv[i] = wq_i(i);
        __builtin_amdgcn_sched_barrier(0);
        if ((i - 4) % 6 != 0) pf[3 + (i - 4) * 5 / 6] = pf_i(3 + (i - 4) * 5 / 6);
        __builtin_amdgcn_sched_barrier(0);
      }
      asm volatile("s_barrier" ::: "memory");
#pragma unroll
      for (int i = 13; i < NPF; ++i) pf[i] = pf_i(i);
      __builtin_amdgcn_sched_barrier(0);
#pragma unroll
      for (int t = 0; t < 2; ++t) {
        EV[t] = __builtin_amdgcn_raw_buffer_load_b128(rv, ve + t * 8192, 0, AUX_NT);
        E[t] = __builtin_amdgcn_raw_buffer_load_b128(rk, ve + t * 8192, 0, AUX_NT);
      }
      __builtin_amdgcn_sched_barrier(0);
      float a0[8];
#pragma unroll
      for (int k = 0; k < 8; ++k) a0[k] = 0.f;
#pragma unroll
      for (int jj = 0; jj < 8; ++jj)
#pragma unroll
        for (int k = 0; k < 4; ++k) {
          a0[2 * k]     = dot2(perm_lo(wv[2 * jj][k], wv[2 * jj + 1][k]), xp[jj], a0[2 * k]);
          a0[2 * k + 1] = dot2(perm_hi(wv[2 * jj][k], wv[2 * jj + 1][k]), xp[jj], a0[2 * k + 1]);
        }
      PSTAMP(44);
#pragma unroll
      for (int k = 0; k < 8; ++k) a0[k] = xrow16_sum(a0[k]);
      if (r == 0) {
        *reinterpret_cast<f32x4*>(red + wave * 128 + 8 * c)     = f32x4{a0[0], a0[1], a0[2], a0[3]};
        *reinterpret_cast<f32x4*>(red + wave * 128 + 8 * c + 4) = f32x4{a0[4], a0[5], a0[6], a0[7]};
      }
      bar();
      if (tid < 128) {
        float s = 0.f;
#pragma unroll
        for (int w = 0; w < 8; ++w) s += red[w * 128 + tid];
        qv[tid] = (u16)bf16bits(s);
      }
      bar();
#pragma unroll
      for (int t = 0; t < 4; ++t) A_ROW(t, rk, vo);
#pragma unroll
      for (int i = 0; i < NPF; ++i) asm volatile("" :: "v"(pf[i]));
      PSTAMP(60);
    }
    STAMP(1);
    const u32x4 q4 = ld16(qv + 8 * c);
    float* sc = reinterpret_cast<float*>(smem + LA_SC) + wave * 22 * 64 + lane;
    float* ml = reinterpret_cast<float*>(smem + LA_ML);
    float* av = reinterpret_cast<float*>(smem + LA_ACC);
    float se;
    {
      float x[4] = {0.f, 0.f, 0.f, 0.f};
#pragma unroll
      for (int t = 0; t < 2; ++t) x[t] = dot2(E[t][3], q4[3], dot2(E[t][2], q4[2], dot2(E[t][1], q4[1], dot2(E[t][0], q4[0], 0.f))));
      const float z = score_log2(transpose_reduce4(x, c));
      se = wave + 8 * (c >> 2) < 4 * (e1 - e0) ? z : -INFINITY;
    }
    sc[21 * 64] = se;
    float m2 = se;
    u32 now = (u32)t_start << 4, tgt = now + GT0 * 16 / 10 + (u32)(((b + (h & 6)) * 8 + wave) * 37 % 64) * 5;
    int lag = 0, hold = 0;
    auto look = [&]() {
      now = (u32)__builtin_amdgcn_s_memrealtime() << 4;
      lag = (int)(now - tgt);
      hold = HOLD * 16 / 10 - max(lag, 0) * HOLD / LATE0;
    };
    auto gate = [&](u32 step) {
      tgt += step;
      asm volatile("s_cmp_lt_i32 %0, %1\n\ts_cbranch_scc0 1f\n\ts_waitcnt vmcnt(3)\n1:\n\t"
                   "s_cmp_lt_i32 %0, %2\n\ts_cbranch_scc0 2f\n\ts_waitcnt vmcnt(4)\n2:\n\ts_waitcnt vmcnt(5)"
                   :: "s"(lag), "n"(LATE1 * 16 / 10), "n"(LATE2 * 16 / 10) : "memory", "scc");
      u32 t2 = tgt;
      if (hold > 0) {
        now = (u32)__builtin_amdgcn_s_memrealtime() << 4;
        if ((int)(now + hold - t2) > 0) t2 = now + hold;
      }
      while ((int)(now - t2) < 0) {
        now = (u32)__builtin_amdgcn_s_memrealtime() << 4;
        if ((int)(now - t2) < 0) __builtin_amdgcn_s_sleep(1);
      }
    };
#pragma unroll
    for (int t = 0; t < 4; ++t) { gate(GK * 16 / 40); A_ROW(4 + t, rk, vo + WIN); }
    using I0 = std::integral_constant<int, 0>;
    using I4 = std::integral_constant<int, 4>;
    u32 todo = (1u << 21) - 4, pos = 2, wi[2] = {0, 1};
    auto k_page = [&](auto half, int i, rsrc_t rn, u32 step) {
      constexpr int o = decltype(half)::value;
      look();
      const u32 wc = wi[o / 4];
      u32 w = i - 19;
      if (i < 19) {
        const u32 up = i + 2 + (SYN ? (u32)(max(lag + RUP * 16 / 10, 0) * 50) >> 16 : 0), dn = i + 2 + (SYN ? (u32)(max(lag + RDN * 16 / 10, 0) * 50) >> 16 : 0);
        w = max(up, pos);
        if (dn < pos || w > 20) w = (u32)__builtin_ctz(todo);
        todo &= ~(1u << w);
        pos = max(pos, w + 1);
        wi[o / 4] = w;
      }
      const u32 on = vo + w * WIN;
      float x[4];
#pragma unroll
      for (int t = 0; t < 4; ++t) {
        float d = dot2(R[o + t][0], q4[0], 0.f);
        d = dot2(R[o + t][1], q4[1], d); d = dot2(R[o + t][2], q4[2], d); d = dot2(R[o + t][3], q4[3], d);
        asm volatile("" : "+v"(d) :: "memory");
        x[t] = d;
        gate(step);
        A_ROW(o + t, rn, on);
      }
      PSTAMP(i);
      const float sl = score_log2(transpose_reduce4(x, c));
      m2 = fmaxf(m2, sl);
      sc[wc * 64] = sl;
    };
#pragma unroll 1
    for (int i = 0; i < 20; i += 2) {
      k_page(I0{}, i, rk, GK * 16 / 40);
      k_page(I4{}, i + 1, i == 18 ? rv : rk, GK * 16 / 40);
    }
    tgt += GKV * 16 / 10;
    k_page(I0{}, 20, rv, GV * 16 / 40);
    STAMP(2);
    float acc[8], lsum = 0.f;
    {
      m2 = xrow16_max(row16_max(m2));
      const float pe = __builtin_amdgcn_exp2f(se - m2), p0_ = dpp<BCAST + 0>(pe), p1_ = dpp<BCAST + 4>(pe);
#pragma unroll
      for (int k = 0; k < 4; ++k) {
        acc[2 * k]     = fmaf(p1_, bflo(EV[1][k]), p0_ * bflo(EV[0][k]));
        acc[2 * k + 1] = fmaf(p1_, bfhi(EV[1][k]), p0_ * bfhi(EV[0][k]));
      }
    }
    auto v_page = [&](auto half, int i, bool more) {
      constexpr int o = decltype(half)::value;
      const u32 step = i + 1 >= GD0 && i + 1 < GD1 ? (GV + GD) * 16 / 40 : GV * 16 / 40;
      if (more) look();
      const float p = __builtin_amdgcn_exp2f(sc[i * 64] - m2);
      const float pb[4] = {dpp<BCAST + 0>(p), dpp<BCAST + 4>(p), dpp<BCAST + 8>(p), dpp<BCAST + 12>(p)};
#pragma unroll
      for (int t = 0; t < 4; ++t) {
#pragma unroll
        for (int k = 0; k < 4; ++k) {
          acc[2 * k]     = fmaf(pb[t], bflo(R[o + t][k]), acc[2 * k]);
          acc[2 * k + 1] = fmaf(pb[t], bfhi(R[o + t][k]), acc[2 * k + 1]);
        }
        asm volatile("" : "+v"(acc[0]), "+v"(acc[1]), "+v"(acc[2]), "+v"(acc[3]), "+v"(acc[4]), "+v"(acc[5]), "+v"(acc[6]),
                          "+v"(acc[7]) :: "memory");
        if (more) { gate(step); A_ROW(o + t, rv, vo + (u32)(i + 2) * WIN); }
      }
      if (more && LPP * i < 22) {
#pragma unroll
        for (int k = 0; k < LPP; ++k) lsum += __builtin_amdgcn_exp2f((LPP * i + k < 22 ? sc[(LPP * i + k) * 64] : -INFINITY) - m2);
      }
      PSTAMP(21 + i);
    };
#pragma unroll 1
    for (int i = 0; i < 18; i += 2) {
      v_page(I4{}, i, true);
      v_page(I0{}, i + 1, true);
      if (i != (21 / LPP & ~1)) continue;
      lsum = xrow16_sum(row16_sum(lsum)) * 0.25f;
      u32 n = 0;
      if (lane == 0) { ml[wave] = m2; ml[8 + wave] = lsum; n = arrive(lds0 + LA_ML + 96); }
      if (__builtin_amdgcn_readfirstlane(n) == 7) {
        const float mv = ml[cc], lv = ml[8 + cc];
        float M = fmaxf(mv, dpp<XOR1>(mv)); M = fmaxf(M, dpp<XOR2>(M)); M = fmaxf(M, dpp<HMIR>(M));
        const float f = __builtin_amdgcn_exp2f(mv - M);
        float L = f * lv; L += dpp<XOR1>(L); L += dpp<XOR2>(L); L += dpp<HMIR>(L);
        if (lane < 8) ml[16 + lane] = f;
        if (lane == 0) stc128(rs, blk + 768 + 16 * part, u32x4{asu(M), asu(L), asu(M) ^ asu(L) ^ tag32, tag32});
      }
    }
    v_page(I4{}, 18, true);
    v_page(I0{}, 19, false);
    v_page(I4{}, 20, false);
    WSTAMP(8);
    {
      float u4[4], u2[2];
#pragma unroll
      for (int k = 0; k < 4; ++k) {
        auto t = __builtin_amdgcn_permlane32_swap(asu(acc[k]), asu(acc[4 + k]), false, false);
        u4[k] = asf(t[0]) + asf(t[1]);
      }
#pragma unroll
      for (int k = 0; k < 2; ++k) {
        auto t = __builtin_amdgcn_permlane16_swap(asu(u4[k]), asu(u4[2 + k]), false, false);
        u2[k] = asf(t[0]) + asf(t[1]);
      }
      *reinterpret_cast<f32x2*>(av + wave * 128 + 8 * c + 2 * r) = f32x2{u2[0], u2[1]};
    }
    {
      u32 n = 0;
      if (lane == 0) n = arrive_nw(lds0 + LA_ML + 100);
      f32x2 t[8];
#pragma unroll
      for (int w = 0; w < 8; ++w) t[w] = *reinterpret_cast<const f32x2*>(av + w * 128 + 2 * lane);
      const f32x4 f0 = *reinterpret_cast<const f32x4*>(ml + 16), f1 = *reinterpret_cast<const f32x4*>(ml + 20);
      const f32x2 o = ((t[0] * f0[0] + t[1] * f0[1]) + (t[2] * f0[2] + t[3] * f0[3])) + ((t[4] * f1[0] + t[5] * f1[1]) + (t[6] * f1[2] + t[7] * f1[3]));
      u32 e = min(pack2(o[0], o[1]), VOID - 1);
      asm volatile("" : "+v"(e), "+v"(n));
      if (__builtin_amdgcn_readfirstlane(n) == 7) {
        const u32x4 d = {dppu<0x00>(e), dppu<0x55>(e), dppu<0xAA>(e), dppu<0xFF>(e)};
        stc128(rs, (lane & 3) == 0 ? blk + part * 256 + 4 * lane : OOB, d);
        LSTAMP(3);
      }
    }
#undef A_ROW
    PDUMP();
    return;
  }

  const int col = xcd * 4 + (slot & 3), bsel = slot >> 2;
  float* red = reinterpret_cast<float*>(smem + LH_RED);
  auto slice_load = [&](const u16* m, int cg) {
    const u16* sp = m + (size_t)(512 * wave + (lane >> 1)) * 512 + 16 * cg + 8 * (lane & 1);
#pragma unroll
    for (int i = 0; i < 16; ++i) {
      const int i2 = i ^ (4 * (slot & 3));
      glds16<0>(sp + (size_t)i2 * 32 * 512, lds0 + LH_WO + wave * 16384 + i2 * 1024);
    }
  };
  u32x4 wf[4][4];
  auto slice_frags = [&]() {
    LAS char* img = (LAS char*)(size_t)(lds0 + LH_WO + wave * 16384);
    const u32 q_ = (lane & 15) >> 2, pp = lane & 3;
#pragma unroll
    for (int hd = 0; hd < 4; ++hd)
#pragma unroll
      for (int ks = 0; ks < 4; ++ks) {
        s16x4 t[2];
#pragma unroll
        for (int tt = 0; tt < 2; ++tt)
          t[tt] = __builtin_amdgcn_ds_read_tr16_b64_v4i16((LAS s16x4*)(img + (128 * hd + 32 * ks + 8 * r + 4 * tt + q_) * 32 + 8 * pp));
        wf[hd][ks] = __builtin_bit_cast(u32x4, s16x8{t[0][0], t[0][1], t[0][2], t[0][3], t[1][0], t[1][1], t[1][2], t[1][3]});
      }
  };
  if (slot >= 4) {
    const int chunk = xcd * 4 + slot - 4, b = chunk >> 4;
    const u16* csrc = ckv + (size_t)chunk * 512 * DC;
    const rsrc_t rc = __builtin_amdgcn_make_buffer_rsrc(const_cast<u16*>(csrc), 0, 512 * DC * 2, 0x00020000);
    const int t0_ = 2 * (chunk & 3) + (xcd & 2);
    u32x4 stg[8];
    {
      u32x4 wv[16], xs[2][2];
#pragma unroll
      for (int i = 0; i < 4; ++i) xs[i >> 1][i & 1] = ld16(bx + (i >> 1) * DM + 64 * wave + 16 * r + 8 * (i & 1));
      if (tid < 2) stc64(rs, tid == 0 ? FL_B + (u32)chunk * 128 : WS_QB + (u32)chunk * 1024, u32x2{0u, 0u});
      asm volatile("" ::: "memory");
      const u16* wp = bwq + (size_t)(64 * wave + 16 * r) * 4096 + chunk * DH + 8 * c;
#pragma unroll
      for (int i = 0; i < 16; ++i) wv[i] = ld_nt(wp + (size_t)i * 4096);
      asm volatile("s_barrier" ::: "memory");
#pragma unroll
      for (int i = 0; i < 8; ++i) {
        const u32 row = 8 * wave + i;
        stg[i] = __builtin_amdgcn_raw_buffer_load_b128(rc, ((u32)lane ^ cswz(row & 15)) << 4, (64 * ((2 + t0_) & 7) + row) * 1024, AUX_NT);
      }
      float a0[8], a1[8];
#pragma unroll
      for (int k = 0; k < 8; ++k) { a0[k] = 0.f; a1[k] = 0.f; }
#pragma unroll
      for (int jj = 0; jj < 8; ++jj) {
        const u32 x0 = xs[0][jj >> 2][jj & 3], x1 = xs[1][jj >> 2][jj & 3];
#pragma unroll
        for (int k = 0; k < 4; ++k) {
          const u32 lo = perm_lo(wv[2 * jj][k], wv[2 * jj + 1][k]), hi = perm_hi(wv[2 * jj][k], wv[2 * jj + 1][k]);
          a0[2 * k] = dot2(lo, x0, a0[2 * k]); a0[2 * k + 1] = dot2(hi, x0, a0[2 * k + 1]);
          a1[2 * k] = dot2(lo, x1, a1[2 * k]); a1[2 * k + 1] = dot2(hi, x1, a1[2 * k + 1]);
        }
      }
#pragma unroll
      for (int k = 0; k < 8; ++k) { a0[k] = xrow16_sum(a0[k]); a1[k] = xrow16_sum(a1[k]); }
      float* red = reinterpret_cast<float*>(smem + LC_SP);
      if (r == 0) {
        *reinterpret_cast<f32x4*>(red + wave * 256 + 8 * c)           = f32x4{a0[0], a0[1], a0[2], a0[3]};
        *reinterpret_cast<f32x4*>(red + wave * 256 + 8 * c + 4)       = f32x4{a0[4], a0[5], a0[6], a0[7]};
        *reinterpret_cast<f32x4*>(red + wave * 256 + 128 + 8 * c)     = f32x4{a1[0], a1[1], a1[2], a1[3]};
        *reinterpret_cast<f32x4*>(red + wave * 256 + 128 + 8 * c + 4) = f32x4{a1[4], a1[5], a1[6], a1[7]};
      }
      bar();
      if (wave == 0) {
        f32x4 t = *reinterpret_cast<const f32x4*>(red + 4 * lane);
#pragma unroll
        for (int w = 1; w < 8; ++w) t += *reinterpret_cast<const f32x4*>(red + w * 256 + 4 * lane);
        const u32 v0 = pack2(t[0], t[1]), v1 = pack2(t[2], t[3]);
        stc128(rs, WS_QB + (u32)chunk * 1024 + 16 * lane, u32x4{v0, v0 ^ tag32, v1, v1 ^ tag32});
      }
      STAMP(1);
    }
#pragma unroll
    for (int st = 0; st < 2; ++st)
#pragma unroll
      for (int i = 0; i < 8; ++i) {
        const u32 row = 8 * wave + i;
        glds16<1>(csrc + (size_t)(64 * ((st + t0_) & 7) + row) * DC + 8 * ((u32)lane ^ cswz(row & 15)), lds0 + LC_RING + st * 65536 + row * 1024);
      }
    if (wave == 0) flags_wait<20>(ws, FL_Q, 4 * xcd + 1, tag, lane);
    bar();
    STAMP(5);
    const int kq = wave & 3, hq = wave >> 2;
    const u32 fsw = cswz((u32)c) << 4;
    u32x4 qh[16], ql[16];
    {
      u32x4 t[8];
#pragma unroll
      for (int i = 0; i < 8; ++i) t[i] = ldc128(rs, WS_QF + (u32)(b * 64 + 8 * wave) * 1024 + 16 * lane, (u32)i * 1024);
      const u32 qa = lds0 + LC_SP + (16 * hq + c) * 1024 + 256 * r + fsw;
#pragma unroll
      for (int hl = 0; hl < 2; ++hl) {
#pragma unroll
        for (int i = 0; i < 4; ++i) {
          const u32 row = 4 * wave + i;
          *(LAS u32x4*)(size_t)(lds0 + LC_SP + row * 1024 + (((u32)lane ^ cswz(row & 15)) << 4)) = t[2 * i + hl];
        }
        if (hl == 0) drain();
        bar();
#pragma unroll
        for (int s = 0; s < 16; ++s) (hl ? ql[s] : qh[s]) = *(const LAS u32x4*)(size_t)(qa ^ (u32)(s << 4));
        bar();
      }
    }
    STAMP(18);
    float mrun = -INFINITY, mgrun = -INFINITY, lsum = 0.f;
    f32x16 acc[2];
#pragma unroll
    for (int ct = 0; ct < 2; ++ct)
#pragma unroll
      for (int v = 0; v < 16; ++v) acc[ct][v] = 0.f;
    const u32 blk = (lane >> 4) & 1, q_ = (lane & 15) >> 2, pp = lane & 3;
    const u32 a1 = (16 * kq + c) * 1024 + 256 * r + fsw;
    float* mx = reinterpret_cast<float*>(smem + LC_MX);
#pragma unroll 1
    for (int st = 0; st < 8; ++st) {
      const u32 tb = lds0 + LC_RING + (st & 1) * 65536;
      f32x4 sa = {0.f, 0.f, 0.f, 0.f};
      {
        u32x4 af[4];
#pragma unroll
        for (int s = 0; s < 4; ++s) af[s] = *(const LAS u32x4*)(size_t)((tb + a1) ^ (u32)(s << 4));
#pragma unroll
        for (int s = 0; s < 16; ++s) {
          __builtin_amdgcn_sched_barrier(0);
          sa = mfma16(af[s & 3], qh[s], sa);
          sa = mfma16(af[s & 3], ql[s], sa);
          __builtin_amdgcn_sched_barrier(0);
          if (s + 4 < 16) af[s & 3] = *(const LAS u32x4*)(size_t)((tb + a1) ^ (u32)((s + 4) << 4));
        }
      }
      float sl[4];
#pragma unroll
      for (int v = 0; v < 4; ++v) sl[v] = score_log2(sa[v]);
      const float tm = xrow16_max(fmaxf(fmaxf(sl[0], sl[1]), fmaxf(sl[2], sl[3])));
      if (r == 0) mx[(16 * hq + c) * 4 + kq] = tm;
      bar();
      const f32x4 ms = *reinterpret_cast<const f32x4*>(mx + (16 * hq + c) * 4), mg = *reinterpret_cast<const f32x4*>(mx + l31 * 4);
      const float mn = fmaxf(mrun, fmaxf(fmaxf(ms[0], ms[1]), fmaxf(ms[2], ms[3])));
      const float mgn = fmaxf(mgrun, fmaxf(fmaxf(mg[0], mg[1]), fmaxf(mg[2], mg[3])));
      float ps = 0.f;
#pragma unroll
      for (int v = 0; v < 4; ++v) { sl[v] = __builtin_amdgcn_exp2f(sl[v] - mn); ps += sl[v]; }
      lsum = fmaf(lsum, __builtin_amdgcn_exp2f(mrun - mn), ps);
      mrun = mn;
      *reinterpret_cast<u32x2*>(smem + LC_P + (kq * 64 + (r & 1) * 32 + 16 * hq + c) * 16 + (r >> 1) * 8) =
          u32x2{pack2(sl[0], sl[1]), pack2(sl[2], sl[3])};
      const float alpha = __builtin_amdgcn_exp2f(mgrun - mgn);
      mgrun = mgn;
#pragma unroll
      for (int ct = 0; ct < 2; ++ct)
#pragma unroll
        for (int v = 0; v < 16; ++v) acc[ct][v] *= alpha;
      if (st >= 1 && st < 7) {
        const u32 ob = lds0 + LC_RING + ((st + 1) & 1) * 65536 + wave * 8192 + 16 * lane;
        const u32 so = st < 6 ? (u32)(64 * ((st + 2 + t0_) & 7) + 8 * wave) * 1024 : OOB;
#pragma unroll
        for (int i = 0; i < 8; ++i) {
          *(LAS u32x4*)(size_t)(ob + i * 1024) = stg[i];
          asm volatile("" ::: "memory");
          stg[i] = __builtin_amdgcn_raw_buffer_load_b128(rc, (((u32)lane ^ cswz((8 * wave + i) & 15)) << 4) + so + i * 1024, 0, AUX_NT);
        }
      }
      bar();
#pragma unroll
      for (int ks = 0; ks < 4; ++ks) {
        const u32x4 pf = *reinterpret_cast<const u32x4*>(smem + LC_P + (ks * 64 + lane) * 16);
#pragma unroll
        for (int ct = 0; ct < 2; ++ct) {
          s16x4 t[2];
#pragma unroll
          for (int tt = 0; tt < 2; ++tt) {
            const u32 row = 4 * hh + 8 * tt + q_;
            const u32 pc = 8 * wave + 4 * ct + 2 * blk + (pp >> 1);
            t[tt] = __builtin_amdgcn_ds_read_tr16_b64_v4i16((LAS s16x4*)(size_t)(tb + (16 * ks + row) * 1024 + ((pc ^ cswz(row)) << 4) + 8 * (pp & 1)));
          }
          const s16x8 cf = {t[0][0], t[0][1], t[0][2], t[0][3], t[1][0], t[1][1], t[1][2], t[1][3]};
          acc[ct] = __builtin_amdgcn_mfma_f32_32x32x16_bf16(__builtin_bit_cast(bf16x8, cf), __builtin_bit_cast(bf16x8, pf), acc[ct], 0, 0, 0);
        }
      }
    }
    STAMP(19);
    lsum = xrow16_sum(lsum);
    float* lsb = reinterpret_cast<float*>(smem + LC_LS);
    if (r == 0) lsb[kq * 32 + 16 * hq + c] = lsum;
#pragma unroll
    for (int ct = 0; ct < 2; ++ct)
#pragma unroll
      for (int v4 = 0; v4 < 4; ++v4)
        *reinterpret_cast<u32x2*>(smem + LC_RING + l31 * 1040 + (64 * wave + 32 * ct + 8 * v4 + 4 * hh) * 2) =
            u32x2{pack2(acc[ct][4 * v4], acc[ct][4 * v4 + 1]), pack2(acc[ct][4 * v4 + 2], acc[ct][4 * v4 + 3])};
    bar();
#pragma unroll
    for (int i = 0; i < 4; ++i) {
      const int hd = 8 * i + wave;
      __builtin_amdgcn_raw_buffer_store_b128(*reinterpret_cast<const u32x4*>(smem + LC_RING + hd * 1040 + 16 * lane), rs,
                                             WS_LATP + (u32)(chunk * 32 + hd) * 1024 + 16 * lane, 0, AUX_SC1 | AUX_NT);
    }
    if (tid < 32) stc64(rs, WS_LML + (u32)(chunk * 32 + tid) * 8,
                        u32x2{asu(mgrun), asu((lsb[tid] + lsb[32 + tid]) + (lsb[64 + tid] + lsb[96 + tid]))});
    if (tid < 2) reinterpret_cast<u32*>(smem + LH_W + 128)[tid] = 0u;
    drain();
    bar();
    if (tid == 0) flag_set(ws, FL_B + (u32)chunk * 128, tag);
    STAMP(3);
    slice_load(bwo, col ^ 16);
    drain();
    slice_frags();
#pragma unroll
    for (int hd = 0; hd < 4; ++hd) asm volatile("" : "+v"(wf[hd][0]), "+v"(wf[hd][1]), "+v"(wf[hd][2]), "+v"(wf[hd][3]) :: "memory");
    slice_load(wo, col);
    drain();
    STAMP(23);
  } else {

  const int h = xcd * 4 + slot, hx = 4 * slot;
  const u16* vp = wuv + (size_t)(64 * wave + r) * 4096 + h * DH + 8 * c;
  u32x4 wv[16];
  {
    u16* qv = reinterpret_cast<u16*>(smem + LH_Q);
    u16* qs = reinterpret_cast<u16*>(smem + LH_QS);
    u32x4 wk[16];
    {
      const u16* kp = wuk + (size_t)(64 * wave + r) * 4096 + h * DH + 8 * c;
#pragma unroll
      for (int i = 0; i < 16; ++i) wk[i] = ld_nt(kp + (size_t)(i ^ hx) * 4 * 4096);
      if (tid < 33) stc128(rs, tid == 32 ? FL_Q + (u32)h * 128 : WS_MA + (u32)(64 + h * 2 + r) * 1024 + 16 * c,
                           u32x4{VOID, VOID, VOID, VOID});
      if (tid < 2) reinterpret_cast<u32*>(smem + LH_W + 128)[tid] = 0u;
      asm volatile("" ::: "memory");
#pragma unroll
      for (int i = 0; i < 8; ++i) wv[i] = ld_nt(vp + (size_t)(i ^ hx) * 4 * 4096);
      asm volatile("" ::: "memory");
    }
    if (wave == 0) {
      u32 spins = 0;
      u32x4 g;
      for (;;) {
        g = ldc128(rs, WS_QB + (u32)h * 1024 + 16 * lane);
        if (__all(((g[0] ^ g[1] ^ tag32) | (g[2] ^ g[3] ^ tag32)) == 0u) || timed_out(ws, tag, spins, lane)) break;
        __builtin_amdgcn_s_sleep(4);
      }
      *reinterpret_cast<u32x2*>(qv + 4 * lane) = u32x2{g[0], g[2]};
    }
    bar();
#pragma unroll
    for (int bb = 0; bb < 2; ++bb) {
      const u32x4 q4 = ld16(qv + bb * 128 + 8 * c);
#pragma unroll
      for (int g = 0; g < 4; ++g) {
        float x[4];
#pragma unroll
        for (int t = 0; t < 4; ++t) {
          const u32x4 w4 = wk[4 * g + t];
          float d = dot2(w4[0], q4[0], 0.f);
          d = dot2(w4[1], q4[1], d); d = dot2(w4[2], q4[2], d); d = dot2(w4[3], q4[3], d);
          x[t] = d;
        }
        const float z = transpose_reduce4(x, c);
        if ((c & 3) == 0) {
          const int ci = 64 * wave + 4 * ((4 * g) ^ hx) + 4 * (c >> 2) + r;
          const u32 hi = bf16bits(z);
          qs[(bb * 2 + 0) * 512 + ci] = (u16)hi;
          qs[(bb * 2 + 1) * 512 + ci] = (u16)bf16bits(z - asf(hi << 16));
        }
      }
    }
    bar();
    if (tid < 256)
      stc128(rs, WS_QF + (u32)(((wave >> 1) * 32 + h) * 2 + (wave & 1)) * 1024 + 16 * lane, *reinterpret_cast<const u32x4*>(qs + 8 * tid));
    drain();
    bar();
    if (tid == 0) flag_set(ws, FL_Q + (u32)h * 128, tag);
    asm volatile("" ::: "memory");
#pragma unroll
    for (int i = 8; i < 16; ++i) wv[i] = ld_nt(vp + (size_t)(i ^ hx) * 4 * 4096);
    asm volatile("" ::: "memory");
    STAMP(1);
  }
  {
    slice_load(bwo, col ^ 16);
    drain();
    slice_frags();
#pragma unroll
    for (int hd = 0; hd < 4; ++hd) asm volatile("" : "+v"(wf[hd][0]), "+v"(wf[hd][1]), "+v"(wf[hd][2]), "+v"(wf[hd][3]) :: "memory");
    slice_load(wo, col);
    STAMP(17);
    {
      const rsrc_t rcode = __builtin_amdgcn_make_buffer_rsrc((void*)(pc0 & ~255ull), 0, 32768, 0x00020000);
      const u32x4 c0 = __builtin_amdgcn_raw_buffer_load_b128(rcode, (u32)(slot * 8 + wave) * 1024 + 16 * lane, 0, 0);
      asm volatile("" :: "v"(c0));
    }
    if (wave == 0) flags_wait<40>(ws, FL_B, h, tag, lane);
    bar();
    STAMP(2);
    float* cw = reinterpret_cast<float*>(smem + LH_W);
    float* part_ = reinterpret_cast<float*>(smem + LH_PART);
    float* lat = reinterpret_cast<float*>(smem + LH_LAT);
    u16* ctx = reinterpret_cast<u16*>(smem + LH_CTX);
    {
      const int bb = wave >> 2, qt = wave & 3;
      const u32 lo = WS_LATP + (u32)(4 * wave * 32 + h) * 1024 + 16 * lane;
      u32x4 v[4];
#pragma unroll
      for (int i = 0; i < 4; ++i) v[i] = __builtin_amdgcn_raw_buffer_load_b128(rs, lo, (u32)i * 32 * 1024, AUX_SC1 | AUX_NT);
      if (wave == 0) {
        const u32x2 ml = ldc64(rs, WS_LML + (u32)(l31 * 32 + h) * 8);
        const float mj = asf(ml[0]), lj = asf(ml[1]);
        const float M = row16_max(mj);
        const float w = __builtin_amdgcn_exp2f(mj - M);
        const float L = row16_sum(w * lj);
        cw[l31] = w / L;
      }
      bar();
      float a8[8];
#pragma unroll
      for (int e = 0; e < 8; ++e) a8[e] = 0.f;
#pragma unroll
      for (int i = 0; i < 4; ++i) {
        const float w = cw[4 * wave + i];
#pragma unroll
        for (int e = 0; e < 4; ++e) { a8[2 * e] = fmaf(w, bflo(v[i][e]), a8[2 * e]); a8[2 * e + 1] = fmaf(w, bfhi(v[i][e]), a8[2 * e + 1]); }
      }
      *reinterpret_cast<f32x4*>(part_ + (qt * 2 + bb) * 512 + 8 * lane)     = f32x4{a8[0], a8[1], a8[2], a8[3]};
      *reinterpret_cast<f32x4*>(part_ + (qt * 2 + bb) * 512 + 8 * lane + 4) = f32x4{a8[4], a8[5], a8[6], a8[7]};
      bar();
      lat[tid] = (part_[tid] + part_[1024 + tid]) + (part_[2048 + tid] + part_[3072 + tid]);
      lat[512 + tid] = (part_[512 + tid] + part_[1536 + tid]) + (part_[2560 + tid] + part_[3584 + tid]);
      bar();
    }
    {
      float a0[8], a1[8];
#pragma unroll
      for (int k = 0; k < 8; ++k) { a0[k] = 0.f; a1[k] = 0.f; }
#pragma unroll
      for (int i = 0; i < 16; ++i) {
        const float l0 = lat[64 * wave + 4 * (i ^ hx) + r], l1 = lat[512 + 64 * wave + 4 * (i ^ hx) + r];
#pragma unroll
        for (int k = 0; k < 4; ++k) {
          a0[2 * k] = fmaf(l0, bflo(wv[i][k]), a0[2 * k]); a0[2 * k + 1] = fmaf(l0, bfhi(wv[i][k]), a0[2 * k + 1]);
          a1[2 * k] = fmaf(l1, bflo(wv[i][k]), a1[2 * k]); a1[2 * k + 1] = fmaf(l1, bfhi(wv[i][k]), a1[2 * k + 1]);
        }
      }
#pragma unroll
      for (int k = 0; k < 8; ++k) { a0[k] = xrow16_sum(a0[k]); a1[k] = xrow16_sum(a1[k]); }
      if (r == 0) {
        *reinterpret_cast<f32x4*>(red + wave * 256 + 8 * c)           = f32x4{a0[0], a0[1], a0[2], a0[3]};
        *reinterpret_cast<f32x4*>(red + wave * 256 + 8 * c + 4)       = f32x4{a0[4], a0[5], a0[6], a0[7]};
        *reinterpret_cast<f32x4*>(red + wave * 256 + 128 + 8 * c)     = f32x4{a1[0], a1[1], a1[2], a1[3]};
        *reinterpret_cast<f32x4*>(red + wave * 256 + 128 + 8 * c + 4) = f32x4{a1[4], a1[5], a1[6], a1[7]};
      }
      bar();
      if (tid < 256) {
        float s = 0.f;
#pragma unroll
        for (int w = 0; w < 8; ++w) s += red[w * 256 + tid];
        ctx[tid] = (u16)bf16bits(s);
      }
      bar();
      if (tid < 32) {
        u32x4 d = ld16(ctx + 8 * tid);
#pragma unroll
        for (int k = 0; k < 4; ++k) d[k] = min(d[k], VOID - 1);
        stc128(rs, WS_MA + (u32)(64 + h * 2 + r) * 1024 + 16 * c, d);
      }
    }
    STAMP(4);
  }
  }
#pragma unroll 1
  for (int pa = 0; pa < 2; ++pa) {
    const bool skip = (c & 3) >= (pa ? 3 : 1);
    const u32 b0 = WS_MA + (u32)((pa ? 0 : 64) + 8 * wave + ((c >> 1) & 2) + bsel) * 1024;
    u32 spins = 0;
    float wt[2] = {1.f, 1.f};
    if (pa) {
      slice_frags();
      u32x4 hd[2];
      for (;;) {
        u32 bad = 0;
#pragma unroll
        for (int k = 0; k < 2; ++k) hd[k] = ldc128(rs, skip ? OOB : b0 + 768 + 16 * (c & 3), (u32)k * 4096);
#pragma unroll
        for (int k = 0; k < 2; ++k) bad |= (hd[k][3] ^ tag32) | (hd[k][0] ^ hd[k][1] ^ hd[k][2] ^ tag32);
        if (__all(skip || bad == 0u) || timed_out(ws, tag, spins, lane)) break;
        __builtin_amdgcn_s_sleep(16);
      }
#pragma unroll
      for (int k = 0; k < 2; ++k) {
        const float mm = skip ? -INFINITY : asf(hd[k][0]);
        float M = fmaxf(mm, dpp<XOR1>(mm)); M = fmaxf(M, dpp<XOR2>(M));
        const float ee = __builtin_amdgcn_exp2f(mm - M);
        float L = ee * asf(hd[k][1]); L += dpp<XOR1>(L); L += dpp<XOR2>(L);
        wt[k] = ee * __builtin_amdgcn_rcpf(L);
      }
    } else __builtin_amdgcn_s_sleep(32);
    const u32 od = b0 + (c & 3) * 256 + (c >> 3) * 64 + 16 * r;
    STAMP(20 + pa);
    u32x4 d[2][2];
    f32x4 e[2];
    bool miss[2];
#pragma unroll
    for (int k = 0; k < 2; ++k) { d[k][0] = d[k][1] = u32x4{0u, 0u, 0u, 0u}; e[k] = f32x4{0.f, 0.f, 0.f, 0.f}; miss[k] = !skip; }
    u32 pend = 3;
    for (;;) {
#pragma unroll
      for (int k = 0; k < 2; ++k)
        if (miss[k]) { d[k][0] = ldc128(rs, od, (u32)k * 4096); d[k][1] = ldc128(rs, od + 128, (u32)k * 4096); }
#pragma unroll
      for (int k = 0; k < 2; ++k) {
        if (!((pend >> k) & 1)) continue;
        miss[k] = max(max(max(d[k][0][0], d[k][0][1]), max(d[k][0][2], d[k][0][3])), max(max(d[k][1][0], d[k][1][1]), max(d[k][1][2], d[k][1][3]))) == VOID;
        if (__any(miss[k])) continue;
        pend &= ~(1u << k);
        const f32x4 z = {0.f, 0.f, 0.f, 0.f};
        f32x4 t[4];
#pragma unroll
        for (int q = 0; q < 4; ++q) t[q] = mfma16(wf[2 * k + (q >> 1)][q & 1], d[k][0], z);
#pragma unroll
        for (int q = 0; q < 4; ++q) t[q] = mfma16(wf[2 * k + (q >> 1)][2 + (q & 1)], d[k][1], t[q]);
#pragma unroll
        for (int v = 0; v < 4; ++v) e[k][v] = wt[k] * ((c & 4) ? ((c & 8) ? t[3][v] : t[2][v]) : ((c & 8) ? t[1][v] : t[0][v]));
      }
      if (pend == 0 || timed_out(ws, tag, spins, lane)) break;
      if (pa) __builtin_amdgcn_s_sleep(1); else __builtin_amdgcn_s_sleep(4);
    }
    WSTAMP(8);
    f32x4 y = e[0] + e[1];
#pragma unroll
    for (int v = 0; v < 4; ++v) y[v] = row16_sum(y[v]);
    float* fin = red + 128 * pa;
    if (c == 0) *reinterpret_cast<f32x4*>(fin + wave * 16 + 4 * r) = y;
    u32 n = 0;
    if (lane == 0) n = arrive_nw(lds0 + LH_W + 128 + 4 * pa);
    float t[8];
#pragma unroll
    for (int w = 0; w < 8; ++w) t[w] = fin[w * 16 + c];
    u32 o = bf16bits(((t[0] + t[1]) + (t[2] + t[3])) + ((t[4] + t[5]) + (t[6] + t[7])));
    asm volatile("" : "+v"(o), "+v"(n));
    if (__builtin_amdgcn_readfirstlane(n) == 7) {
      if (lane < 16) out[bsel * 1024 + (pa ? 16 * col : 512 + 16 * (col ^ 16)) + c] = (u16)o;
      LSTAMP(7);
    }
  }
}

extern "C" void kernel_launch(void* const* d_in, const int* in_sizes, int n_in, void* d_out, int out_size, void* d_ws, size_t ws_size,
                              hipStream_t stream) {
  mk_main<<<256, 512, 0, stream>>>((const u16*)d_in[0], (const u16*)d_in[1], (const u16*)d_in[2], (const u16*)d_in[3],
                                   (const u16*)d_in[4], (const u16*)d_in[5], (const u16*)d_in[6], (const u16*)d_in[7],
                                   (const u16*)d_in[8], (const u16*)d_in[9], (const u16*)d_in[10], (u16*)d_out, (char*)d_ws);
}
```

```cpp
#include <hip/hip_runtime.h>
#include <hip/hip_bf16.h>
#include <stdint.h>
#include <type_traits>

typedef unsigned short u16;
typedef unsigned int u32;
typedef unsigned long long u64;
using u32x2  = __attribute__((ext_vector_type(2))) u32;
using u32x4  = __attribute__((ext_vector_type(4))) u32;
using f32x2  = __attribute__((ext_vector_type(2))) float;
using f32x4  = __attribute__((ext_vector_type(4))) float;
using f32x16 = __attribute__((ext_vector_type(16))) float;
using s16x4  = __attribute__((ext_vector_type(4))) short;
using s16x8  = __attribute__((ext_vector_type(8))) short;
using bf16x2 = __attribute__((ext_vector_type(2))) __bf16;
using bf16x8 = __attribute__((ext_vector_type(8))) __bf16;
#define LAS __attribute__((address_space(3)))
#define DEVI __device__ __forceinline__
#define RLX_AGENT __ATOMIC_RELAXED, __HIP_MEMORY_SCOPE_AGENT

constexpr float SCALE = 0.088388347648318440f;
constexpr float LOG2E = 1.4426950408889634f;
constexpr int SK = 8192, DH = 128, DM = 512, DC = 512;

constexpr u32 FL_Q = 0, FL_B = 4096, FL_TMO = 8192, FL_LATE = 10240;
constexpr u32 WS_MA   = 12288;
constexpr u32 VOID = 0xffffffffu;
constexpr u32 WS_QB   = WS_MA + 128 * 1024;
constexpr u32 WS_QF   = WS_QB + 32 * 1024;
constexpr u32 WS_LML  = WS_QF + 2 * 32 * 2 * 512 * 2;
constexpr u32 WS_LATP = WS_LML + 32 * 32 * 2 * 4;
constexpr u32 WS_END  = WS_LATP + 32 * 32 * 512 * 2;
constexpr u32 OOB = 0x80000000u;
constexpr u32 SPIN_CAP = 1u << 17;
#define PSTAMP(i) do { } while (0)
#define PDUMP() do { } while (0)
#define STAMP(i) do { } while (0)
#define LSTAMP(i) do { } while (0)
#define WSTAMP(i) do { } while (0)

constexpr int L_TOTAL = 163840;
constexpr int LA_Q   = 0;
constexpr int LA_ML  = 256;
constexpr int LA_ACC = 512;
constexpr int LA_SC  = 8192;
constexpr int LC_RING = 0;
constexpr int LC_SP   = 131072;
constexpr int LC_MX   = LC_SP;
constexpr int LC_P    = LC_SP + 512;
constexpr int LC_LS   = LC_P + 4096;
constexpr int LH_WO   = 0;
constexpr int LH_RED  = 131072;
constexpr int LH_W    = LH_RED + 8192;
constexpr int LH_CTX  = LH_W + 256;
constexpr int LH_LAT  = LH_CTX + 512;
constexpr int LH_PART = LH_LAT + 4096;
constexpr int LH_Q    = LH_PART;
constexpr int LH_QS   = LH_Q + 512;
static_assert(LH_PART + 16384 <= L_TOTAL, "LDS");

DEVI float dot2(u32 a, u32 b, float c) {
  return __builtin_amdgcn_fdot2_f32_bf16(__builtin_bit_cast(bf16x2, a), __builtin_bit_cast(bf16x2, b), c, false);
}
DEVI u32 bf16bits(float x) { return (u32)__builtin_bit_cast(u16, (__bf16)x); }
DEVI float bf16r(float x) { return __builtin_bit_cast(float, bf16bits(x) << 16); }
DEVI float bflo(u32 v) { return __builtin_bit_cast(float, v << 16); }
DEVI float bfhi(u32 v) { return __builtin_bit_cast(float, v & 0xffff0000u); }
DEVI float asf(u32 v) { return __builtin_bit_cast(float, v); }
DEVI u32 asu(float v) { return __builtin_bit_cast(u32, v); }
DEVI u32 pack2(float lo, float hi) { return __builtin_bit_cast(u32, __builtin_convertvector(f32x2{lo, hi}, bf16x2)); }
template <int CTRL> DEVI float dpp(float x) {
  return __builtin_bit_cast(float, __builtin_amdgcn_mov_dpp(__builtin_bit_cast(int, x), CTRL, 0xf, 0xf, true));
}
template <int CTRL> DEVI u32 dppu(u32 x) { return (u32)__builtin_amdgcn_mov_dpp((int)x, CTRL, 0xf, 0xf, true); }
constexpr int XOR1 = 0xB1, XOR2 = 0x4E, HMIR = 0x141, ROR8 = 0x128, ROR4 = 0x124, BCAST = 0x150;
DEVI float xrow16_sum(float x) {
  auto s = __builtin_amdgcn_permlane16_swap(__float_as_uint(x), __float_as_uint(x), false, false);
  x = __uint_as_float(s[0]) + __uint_as_float(s[1]);
  auto t = __builtin_amdgcn_permlane32_swap(__float_as_uint(x), __float_as_uint(x), false, false);
  return __uint_as_float(t[0]) + __uint_as_float(t[1]);
}
DEVI float xrow16_max(float x) {
  auto s = __builtin_amdgcn_permlane16_swap(__float_as_uint(x), __float_as_uint(x), false, false);
  x = fmaxf(__uint_as_float(s[0]), __uint_as_float(s[1]));
  auto t = __builtin_amdgcn_permlane32_swap(__float_as_uint(x), __float_as_uint(x), false, false);
  return fmaxf(__uint_as_float(t[0]), __uint_as_float(t[1]));
}
DEVI float row16_sum(float x) { x += dpp<XOR1>(x); x += dpp<XOR2>(x); x += dpp<ROR4>(x); x += dpp<ROR8>(x); return x; }
DEVI float row16_max(float x) { x = fmaxf(x, dpp<XOR1>(x)); x = fmaxf(x, dpp<XOR2>(x)); x = fmaxf(x, dpp<ROR4>(x)); x = fmaxf(x, dpp<ROR8>(x)); return x; }
DEVI u32x4 ld_nt(const u16* p) { return __builtin_nontemporal_load(reinterpret_cast<const u32x4*>(p)); }
DEVI u32x4 ld16(const u16* p) { return *reinterpret_cast<const u32x4*>(p); }
DEVI u32 perm_lo(u32 a, u32 b) { return __builtin_amdgcn_perm(b, a, 0x05040100u); }
DEVI u32 perm_hi(u32 a, u32 b) { return __builtin_amdgcn_perm(b, a, 0x07060302u); }
DEVI f32x4 mfma16(const u32x4& a, const u32x4& b, const f32x4& c) {
  return __builtin_amdgcn_mfma_f32_16x16x32_bf16(__builtin_bit_cast(bf16x8, a), __builtin_bit_cast(bf16x8, b), c, 0, 0, 0);
}
DEVI void bar() { asm volatile("s_waitcnt lgkmcnt(0)\n\ts_barrier" ::: "memory"); }
DEVI u32 arrive(u32 a) { u32 n; asm volatile("ds_add_rtn_u32 %0, %1, %2\n\ts_waitcnt lgkmcnt(0)" : "=v"(n) : "v"(a), "v"(1u) : "memory"); return n; }
DEVI u32 arrive_nw(u32 a) { u32 n; asm volatile("ds_add_rtn_u32 %0, %1, %2" : "=v"(n) : "v"(a), "v"(1u) : "memory"); return n; }
DEVI void drain() { asm volatile("s_waitcnt vmcnt(0)" ::: "memory"); }
template <int NT>
DEVI void glds16(const void* g, u32 lds_base) {
  u32 sv;
  if (NT) asm volatile("s_mov_b32 %0, m0\n\ts_mov_b32 m0, %2\n\ts_nop 0\n\tglobal_load_lds_dwordx4 %1, off nt\n\ts_mov_b32 m0, %0"
                       : "=&s"(sv) : "v"(g), "s"(lds_base) : "memory");
  else asm volatile("s_mov_b32 %0, m0\n\ts_mov_b32 m0, %2\n\ts_nop 0\n\tglobal_load_lds_dwordx4 %1, off\n\ts_mov_b32 m0, %0"
                    : "=&s"(sv) : "v"(g), "s"(lds_base) : "memory");
}

typedef __amdgpu_buffer_rsrc_t rsrc_t;
constexpr int AUX_NT = 2, AUX_SC1 = 16;
DEVI u32x4 ldc128(rsrc_t rs, u32 off, u32 soff = 0) { return __builtin_amdgcn_raw_buffer_load_b128(rs, off, soff, AUX_SC1); }
DEVI u32x2 ldc64(rsrc_t rs, u32 off) { return __builtin_amdgcn_raw_buffer_load_b64(rs, off, 0, AUX_SC1); }
DEVI void stc128(rsrc_t rs, u32 off, u32x4 v) { __builtin_amdgcn_raw_buffer_store_b128(v, rs, off, 0, AUX_SC1); }
DEVI void stc64(rsrc_t rs, u32 off, u32x2 v) { __builtin_amdgcn_raw_buffer_store_b64(v, rs, off, 0, AUX_SC1); }
extern "C" __device__ u64 mk_dispatch_id() __asm("llvm.amdgcn.dispatch.id");
DEVI void flag_set(char* ws, u32 off, u64 tag) { __hip_atomic_store(reinterpret_cast<u64*>(ws + off), tag, RLX_AGENT); }
DEVI bool timed_out(char* ws, u64 tag, u32& spins, int lane) {
  if (++spins > SPIN_CAP) { if (lane == 0) flag_set(ws, FL_TMO, tag); return true; }
  return (spins & 255u) == 0u && __hip_atomic_load(reinterpret_cast<u64*>(ws + FL_TMO), RLX_AGENT) == tag;
}
template <int GAP>
DEVI void flags_wait(char* ws, u32 off, int first, u64 tag, int lane) {
  u32 spins = 0;
#pragma unroll 1
  for (int ph = 0; ph < 2; ++ph) {
    const u64* p = reinterpret_cast<const u64*>(ws + off + (ph == 0 ? first : lane & 31) * 128);
    for (;;) {
      if (__all(__hip_atomic_load(p, RLX_AGENT) == tag)) break;
      if (timed_out(ws, tag, spins, lane)) return;
      __builtin_amdgcn_s_sleep(16);
    }
    if (ph == 0) __builtin_amdgcn_s_sleep(GAP);
  }
}

DEVI float transpose_reduce4(const float (&x)[4], int c) {
  const bool h8 = (c & 8) != 0;
  const float s0 = h8 ? x[0] : x[2], k0 = h8 ? x[2] : x[0];
  const float s1 = h8 ? x[1] : x[3], k1 = h8 ? x[3] : x[1];
  const float y0 = k0 + dpp<ROR8>(s0), y1 = k1 + dpp<ROR8>(s1);
  const bool h4 = (c & 4) != 0;
  const float s2 = h4 ? y0 : y1, k2 = h4 ? y1 : y0;
  float z = k2 + dpp<HMIR>(s2);
  z += dpp<XOR2>(z);
  z += dpp<XOR1>(z);
  return z;
}
DEVI float score_log2(float z) { return bf16r(bf16r(z) * SCALE) * LOG2E; }
DEVI u32 cswz(u32 row) { return ((row & 3u) << 2) | ((row >> 2) & 3u); }

#define GT0 6500
#define GK 830
#define GV 800
#define GKV 100
#define GD 100
#define GD0 3
#define GD1 11
#define HOLD 128
#define LATE0 512
#define LATE1 600
#define LATE2 1100
constexpr int CALL0 = 1500, CALLS = 320, REST[4] = {200, 400, 700, 1100};
constexpr int NS = 9, NPF = 17;

__global__ __launch_bounds__(512, 2) void mk_main(const u16* __restrict__ ax, const u16* __restrict__ kc, const u16* __restrict__ vc,
                                                  const u16* __restrict__ wq, const u16* __restrict__ wo, const u16* __restrict__ bx,
                                                  const u16* __restrict__ ckv, const u16* __restrict__ bwq, const u16* __restrict__ wuk,
                                                  const u16* __restrict__ wuv, const u16* __restrict__ bwo, u16* __restrict__ out,
                                                  char* __restrict__ ws) {
  __shared__ __attribute__((aligned(16))) char smem[L_TOTAL];
  const u32 lds0 = (u32)(size_t)(LAS char*)smem;
  int bid = blockIdx.x;
  u64 pc0;
  asm volatile("s_getpc_b64 %0" : "=s"(pc0));
  u64 t_start = __builtin_amdgcn_s_memrealtime();
  asm volatile("" : "+s"(bid));
  const int tid = threadIdx.x, lane = tid & 63, wave = __builtin_amdgcn_readfirstlane(tid >> 6);
  const int r = lane >> 4, c = lane & 15, hh = lane >> 5, l31 = lane & 31, rg = lane >> 3, cc = lane & 7;
  const int xcd = bid & 7, slot = bid >> 3;
  const int a = (slot - 8) & 31, j = a % 6, ab = j / 3, part = j % 3, rot = (4 * j) / 3;
  const int ah = (((xcd + a / 6) & 3) | (xcd & 4)) + 8 * (a / 6);
  u32 wqo = (u32)(64 * wave + 16 * r) * 8192 + ah * 256 + 16 * c;
  u64 tag;
  {
    const u64 did = mk_dispatch_id(), dp = (u64)(size_t)__builtin_amdgcn_dispatch_ptr();
    u32 lo = (u32)did ^ 0x5bd1e995u, hi = (u32)(dp >> 6) ^ (u32)(did >> 32);
    if (lo == hi) hi ^= 0x10000u;
    tag = ((u64)hi << 32) | lo;
  }
  u32 tag32 = ((u32)tag * 0x9E3779B1u) ^ ((u32)(tag >> 32) * 0x85EBCA77u);
  if (((tag32 ^ (tag32 >> 8)) & 0x00ffffffu) == 0u) tag32 ^= 0x00010200u;
  asm volatile("" : "+v"(wqo), "+s"(t_start) : "s"(rot), "s"(part), "s"(ab), "s"(tag32), "s"(ax), "s"(kc), "s"(vc), "s"(wq), "s"(bx),
               "s"(ckv), "s"(bwq), "s"(wuk), "s"(wuv), "s"(bwo), "s"(wo), "s"(ws));
  const rsrc_t rs = __builtin_amdgcn_make_buffer_rsrc(ws, 0, WS_END, 0x00020000);
  STAMP(0);

  if (__builtin_expect(slot >= 8, 1)) {
    PSTAMP(62);
    const int b = ab, h = ah, bh = b * 32 + h;
    const int e0 = (8 * part) / 3, e1 = (8 * (part + 1)) / 3;
    const u32 blk = WS_MA + (u32)(h * 2 + b) * 1024;
    const size_t kvoff = (size_t)bh * SK * DH;
    const rsrc_t rk = __builtin_amdgcn_make_buffer_rsrc(const_cast<u16*>(kc) + kvoff, 0, (504 + e1) * 4096, 0x00020000);
    const rsrc_t rv = __builtin_amdgcn_make_buffer_rsrc(const_cast<u16*>(vc) + kvoff, 0, (504 + e1) * 4096, 0x00020000);
    constexpr u32 WIN = 3 * 32768;
    const u32 vo = (u32)(part * 8 + wave) * 4096 + 16 * lane, ve = (u32)(504 + e0) * 4096 + (u32)wave * 1024 + 16 * lane;
    u32 ro[4];
#pragma unroll
    for (int t = 0; t < 4; ++t) ro[t] = (u32)((t + 2 * b + (h >> 2 & 1)) & 3) * 1024;
    u32x4 R[8], E[2], EV[2];
#define A_ROW(t, rsc, off) do { R[t] = __builtin_amdgcn_raw_buffer_load_b128(rsc, off, ro[(t) & 3], AUX_NT); __builtin_amdgcn_sched_barrier(0); } while (0)
    float* red = reinterpret_cast<float*>(smem + LA_SC);
    u16* qv = reinterpret_cast<u16*>(smem + LA_Q);
    {
      const rsrc_t rw = __builtin_amdgcn_make_buffer_rsrc(const_cast<u16*>(wq), 0, 512 * 4096 * 2, 0x00020000);
      u32x4 wv[16];
      u32 xp[8];
      auto wq_i = [&](int i) -> u32x4 {
        const u32 so = (u32)(2 * (((i >> 1) + rot) & 7) + (i & 1)) * 8192;
        return i < 4 ? __builtin_amdgcn_raw_buffer_load_b128(rw, wqo, so, 0) : __builtin_amdgcn_raw_buffer_load_b128(rw, wqo, so, AUX_NT);
      };
#pragma unroll
      for (int i = 0; i < 4; ++i) wv[i] = wq_i(i);
      const u32* xq = reinterpret_cast<const u32*>(ax) + b * 256 + 32 * wave + 8 * r;
#pragma unroll
      for (int jj = 0; jj < 8; ++jj) xp[jj] = xq[(jj + rot) & 7];
      if (tid < 17) stc128(rs, tid == 16 ? blk + 768 + 16 * part : blk + part * 256 + 16 * tid, u32x4{VOID, VOID, VOID, VOID});
      if (tid < 2) reinterpret_cast<u32*>(smem + LA_ML)[24 + tid] = 0u;
      asm volatile("s_barrier" ::: "memory");
      u32x4 pf[NPF];
      const int W = a * 8 + wave;
      const int slow = (1 - xcd) & 3;
      const rsrc_t rc = __builtin_amdgcn_make_buffer_rsrc(const_cast<u16*>(ckv) + (size_t)xcd * 4 * 512 * DC, 0, 4 * 512 * DC * 2, 0x00020000);
      auto pf_c = [&](int L) {
        const bool one = L >= 512 && L < 768;
        const int M = L >= 768 ? L + 256 : L, q = (one ? L >> 6 : M >> 7) & 3, tl = one ? 3 : 2 * (M >> 9) + ((M >> 6) & 1);
        const int row = (L & 32) | (((L + (L >> 3) + (L >> 6)) & 7) << 2) | ((L >> 3) & 3);
        return (u32)(q * 512 + 64 * ((tl + 2 * q + (xcd & 2)) & 7) + row) * 1024 + 16 * lane;
      };
      auto pf_i = [&](int i) -> u32x4 {
        if (i >= NS) {
          const int L = NS * 192 - 1024 + 144 * (i - NS) + W - (a / 6 > slow ? 48 : 0);
          return __builtin_amdgcn_raw_buffer_load_b128(rc, a / 6 == slow || L >= 1792 ? OOB : pf_c(L), 0, 0);
        }
        const int L = W + 192 * i, Lw = L - 512;
        if (i < 2 || i >= 8) return __builtin_amdgcn_raw_buffer_load_b128(rc, pf_c(i < 2 ? L : L - 1024), 0, 0);
        const int ln = Lw < 512 ? xcd ^ 4 : xcd;
        const int G = ((Lw & 4) << 6) | ((Lw >> 1) & 0xfc) | (Lw & 3);
        const u16* pw = ln == 3 && G < 256 ? ax : (Lw < 512 ? bwo : wo) + (size_t)(8 * G + rg) * 512 + 64 * ln + 8 * cc;
        return ld16(i > 2 || L >= 512 ? pw : ckv + (size_t)xcd * 4 * 512 * DC + pf_c(L) / 2);
      };
#pragma unroll
      for (int i = 0; i < 3; ++i) pf[i] = pf_i(i);
      __builtin_amdgcn_sched_barrier(0);
#pragma unroll
      for (int i = 4; i < 16; ++i) {
        wv[i] = wq_i(i);
        __builtin_amdgcn_sched_barrier(0);
        if ((i - 4) % 6 != 0) pf[3 + (i - 4) * 5 / 6] = pf_i(3 + (i - 4) * 5 / 6);
        __builtin_amdgcn_sched_barrier(0);
      }
      asm volatile("s_barrier" ::: "memory");
#pragma unroll
      for (int i = 13; i < NPF; ++i) pf[i] = pf_i(i);
      __builtin_amdgcn_sched_barrier(0);
#pragma unroll
      for (int t = 0; t < 2; ++t) {
        EV[t] = __builtin_amdgcn_raw_buffer_load_b128(rv, ve + t * 8192, 0, AUX_NT);
        E[t] = __builtin_amdgcn_raw_buffer_load_b128(rk, ve + t * 8192, 0, AUX_NT);
      }
      __builtin_amdgcn_sched_barrier(0);
      float a0[8];
#pragma unroll
      for (int k = 0; k < 8; ++k) a0[k] = 0.f;
#pragma unroll
      for (int jj = 0; jj < 8; ++jj)
#pragma unroll
        for (int k = 0; k < 4; ++k) {
          a0[2 * k]     = dot2(perm_lo(wv[2 * jj][k], wv[2 * jj + 1][k]), xp[jj], a0[2 * k]);
          a0[2 * k + 1] = dot2(perm_hi(wv[2 * jj][k], wv[2 * jj + 1][k]), xp[jj], a0[2 * k + 1]);
        }
      PSTAMP(44);
      asm volatile("" : "+v"(a0[0]), "+v"(a0[1]), "+v"(a0[2]), "+v"(a0[3]), "+v"(a0[4]), "+v"(a0[5]), "+v"(a0[6]), "+v"(a0[7]) :: "memory");
#pragma unroll
      for (int t = 0; t < 2; ++t) A_ROW(t, rk, vo);
#pragma unroll
      for (int k = 0; k < 8; ++k) a0[k] = xrow16_sum(a0[k]);
      if (r == 0) {
        *reinterpret_cast<f32x4*>(red + wave * 128 + 8 * c)     = f32x4{a0[0], a0[1], a0[2], a0[3]};
        *reinterpret_cast<f32x4*>(red + wave * 128 + 8 * c + 4) = f32x4{a0[4], a0[5], a0[6], a0[7]};
      }
      bar();
      if (tid < 128) {
        float s = 0.f;
#pragma unroll
        for (int w = 0; w < 8; ++w) s += red[w * 128 + tid];
        qv[tid] = (u16)bf16bits(s);
      }
      bar();
#pragma unroll
      for (int t = 2; t < 4; ++t) A_ROW(t, rk, vo);
#pragma unroll
      for (int i = 0; i < NPF; ++i) asm volatile("" :: "v"(pf[i]));
      PSTAMP(60);
    }
    STAMP(1);
    const u32x4 q4 = ld16(qv + 8 * c);
    float* sc = reinterpret_cast<float*>(smem + LA_SC) + wave * 22 * 64 + lane;
    float* ml = reinterpret_cast<float*>(smem + LA_ML);
    float* av = reinterpret_cast<float*>(smem + LA_ACC);
    float se;
    {
      float x[4] = {0.f, 0.f, 0.f, 0.f};
#pragma unroll
      for (int t = 0; t < 2; ++t) x[t] = dot2(E[t][3], q4[3], dot2(E[t][2], q4[2], dot2(E[t][1], q4[1], dot2(E[t][0], q4[0], 0.f))));
      const float z = score_log2(transpose_reduce4(x, c));
      se = wave + 8 * (c >> 2) < 4 * (e1 - e0) ? z : -INFINITY;
    }
    sc[21 * 64] = se;
    float m2 = se;
    u32 now = (u32)t_start << 4, tgt = now + GT0 * 16 / 10 + (u32)(((b + (h & 6)) * 8 + wave) * 37 % 64) * 5;
    int lag = 0, hold = 0;
    auto look = [&]() {
      now = (u32)__builtin_amdgcn_s_memrealtime() << 4;
      lag = (int)(now - tgt);
      hold = HOLD * 16 / 10 - max(lag, 0) * HOLD / LATE0;
    };
    auto gate = [&](u32 step) {
      tgt += step;
      asm volatile("s_cmp_lt_i32 %0, %1\n\ts_cbranch_scc0 1f\n\ts_waitcnt vmcnt(3)\n1:\n\t"
                   "s_cmp_lt_i32 %0, %2\n\ts_cbranch_scc0 2f\n\ts_waitcnt vmcnt(4)\n2:\n\ts_waitcnt vmcnt(5)"
                   :: "s"(lag), "n"(LATE1 * 16 / 10), "n"(LATE2 * 16 / 10) : "memory", "scc");
      u32 t2 = tgt;
      if (hold > 0) {
        now = (u32)__builtin_amdgcn_s_memrealtime() << 4;
        if ((int)(now + hold - t2) > 0) t2 = now + hold;
      }
      while ((int)(now - t2) < 0) {
        now = (u32)__builtin_amdgcn_s_memrealtime() << 4;
        if ((int)(now - t2) < 0) __builtin_amdgcn_s_sleep(1);
      }
    };
#pragma unroll
    for (int t = 0; t < 4; ++t) { gate(GK * 16 / 40); A_ROW(4 + t, rk, vo + WIN); }
    using I0 = std::integral_constant<int, 0>;
    using I4 = std::integral_constant<int, 4>;
    u32x4 calls = {0u, 0u, 0u, 0u};
    u32 todo = (1u << 21) - 4, pos = 2, wi[2] = {0, 1};
    auto k_page = [&](auto half, int i, rsrc_t rn, u32 step) {
      constexpr int o = decltype(half)::value;
      look();
      const u32 wc = wi[o / 4];
      u32 w = i - 19;
      if (i < 19) {
        const u32 up = i + 2 + ((u32)(max(lag + 320, 0) * 50) >> 16), dn = i + 2 + ((u32)(max(lag + 960, 0) * 50) >> 16);
        w = max(up, pos);
        if (dn < pos || w > 20) w = (u32)__builtin_ctz(todo);
        todo &= ~(1u << w);
        pos = max(pos, w + 1);
        wi[o / 4] = w;
      }
      const u32 on = vo + w * WIN;
      float x[4];
#pragma unroll
      for (int t = 0; t < 4; ++t) {
        float d = dot2(R[o + t][0], q4[0], 0.f);
        d = dot2(R[o + t][1], q4[1], d); d = dot2(R[o + t][2], q4[2], d); d = dot2(R[o + t][3], q4[3], d);
        asm volatile("" : "+v"(d) :: "memory");
        x[t] = d;
        gate(step);
        A_ROW(o + t, rn, on);
      }
      PSTAMP(i);
      const float sl = score_log2(transpose_reduce4(x, c));
      m2 = fmaxf(m2, sl);
      sc[wc * 64] = sl;
    };
#pragma unroll 1
    for (int i = 0; i < 20; i += 2) {
      if (i == 10) calls = ldc128(rs, FL_LATE);
      k_page(I0{}, i, rk, GK * 16 / 40);
      if (i == 4 && wave == 0 && lag > CALL0 * 16 / 10)
        __builtin_amdgcn_raw_buffer_store_b32(tag32, rs, lane == 0 ? FL_LATE + 4 * min((lag - CALL0 * 16 / 10) / (CALLS * 16 / 10), 3) : OOB, 0, AUX_SC1);
      if (i == 12) {
        u32 rest = 0;
#pragma unroll
        for (int k = 0; k < 4; ++k) if (__builtin_amdgcn_readfirstlane(calls[k]) == tag32) rest = REST[k] * 16 / 10;
        tgt += rest;
      }
      k_page(I4{}, i + 1, i == 18 ? rv : rk, GK * 16 / 40);
    }
    tgt += GKV * 16 / 10;
    k_page(I0{}, 20, rv, GV * 16 / 40);
    STAMP(2);
    float acc[8], lsum = 0.f;
    {
      m2 = xrow16_max(row16_max(m2));
      const float pe = __builtin_amdgcn_exp2f(se - m2), p0_ = dpp<BCAST + 0>(pe), p1_ = dpp<BCAST + 4>(pe);
#pragma unroll
      for (int k = 0; k < 4; ++k) {
        acc[2 * k]     = fmaf(p1_, bflo(EV[1][k]), p0_ * bflo(EV[0][k]));
        acc[2 * k + 1] = fmaf(p1_, bfhi(EV[1][k]), p0_ * bfhi(EV[0][k]));
      }
    }
    auto v_page = [&](auto half, int i, bool more) {
      constexpr int o = decltype(half)::value;
      const u32 step = i + 1 >= GD0 && i + 1 < GD1 ? (GV + GD) * 16 / 40 : GV * 16 / 40;
      if (more) look();
      const float p = __builtin_amdgcn_exp2f(sc[i * 64] - m2);
      const float pb[4] = {dpp<BCAST + 0>(p), dpp<BCAST + 4>(p), dpp<BCAST + 8>(p), dpp<BCAST + 12>(p)};
#pragma unroll
      for (int t = 0; t < 4; ++t) {
#pragma unroll
        for (int k = 0; k < 4; ++k) {
          acc[2 * k]     = fmaf(pb[t], bflo(R[o + t][k]), acc[2 * k]);
          acc[2 * k + 1] = fmaf(pb[t], bfhi(R[o + t][k]), acc[2 * k + 1]);
        }
        asm volatile("" : "+v"(acc[0]), "+v"(acc[1]), "+v"(acc[2]), "+v"(acc[3]), "+v"(acc[4]), "+v"(acc[5]), "+v"(acc[6]),
                          "+v"(acc[7]) :: "memory");
        if (more) { gate(step); A_ROW(o + t, rv, vo + (u32)(i + 2) * WIN); }
      }
      if (more && i < 6) {
#pragma unroll
        for (int k = 0; k < 4; ++k) lsum += __builtin_amdgcn_exp2f((4 * i + k < 22 ? sc[(4 * i + k) * 64] : -INFINITY) - m2);
      }
      PSTAMP(21 + i);
    };
#pragma unroll 1
    for (int i = 0; i < 18; i += 2) {
      v_page(I4{}, i, true);
      v_page(I0{}, i + 1, true);
      if (i != 4) continue;
      lsum = xrow16_sum(row16_sum(lsum)) * 0.25f;
      u32 n = 0;
      if (lane == 0) { ml[wave] = m2; ml[8 + wave] = lsum; n = arrive(lds0 + LA_ML + 96); }
      if (__builtin_amdgcn_readfirstlane(n) == 7) {
        const float mv = ml[cc], lv = ml[8 + cc];
        float M = fmaxf(mv, dpp<XOR1>(mv)); M = fmaxf(M, dpp<XOR2>(M)); M = fmaxf(M, dpp<HMIR>(M));
        const float f = __builtin_amdgcn_exp2f(mv - M);
        float L = f * lv; L += dpp<XOR1>(L); L += dpp<XOR2>(L); L += dpp<HMIR>(L);
        if (lane < 8) ml[16 + lane] = f;
        if (lane == 0) stc128(rs, blk + 768 + 16 * part, u32x4{asu(M), asu(L), asu(M) ^ asu(L) ^ tag32, tag32});
      }
    }
    v_page(I4{}, 18, true);
    v_page(I0{}, 19, false);
    v_page(I4{}, 20, false);
    WSTAMP(8);
    {
      float u4[4], u2[2];
#pragma unroll
      for (int k = 0; k < 4; ++k) {
        auto t = __builtin_amdgcn_permlane32_swap(asu(acc[k]), asu(acc[4 + k]), false, false);
        u4[k] = asf(t[0]) + asf(t[1]);
      }
#pragma unroll
      for (int k = 0; k < 2; ++k) {
        auto t = __builtin_amdgcn_permlane16_swap(asu(u4[k]), asu(u4[2 + k]), false, false);
        u2[k] = asf(t[0]) + asf(t[1]);
      }
      *reinterpret_cast<f32x2*>(av + wave * 128 + 8 * c + 2 * r) = f32x2{u2[0], u2[1]};
    }
    {
      u32 n = 0;
      if (lane == 0) n = arrive_nw(lds0 + LA_ML + 100);
      f32x2 t[8];
#pragma unroll
      for (int w = 0; w < 8; ++w) t[w] = *reinterpret_cast<const f32x2*>(av + w * 128 + 2 * lane);
      const f32x4 f0 = *reinterpret_cast<const f32x4*>(ml + 16), f1 = *reinterpret_cast<const f32x4*>(ml + 20);
      const f32x2 o = ((t[0] * f0[0] + t[1] * f0[1]) + (t[2] * f0[2] + t[3] * f0[3])) + ((t[4] * f1[0] + t[5] * f1[1]) + (t[6] * f1[2] + t[7] * f1[3]));
      u32 e = min(pack2(o[0], o[1]), VOID - 1);
      asm volatile("" : "+v"(e), "+v"(n));
      if (__builtin_amdgcn_readfirstlane(n) == 7) {
        const u32x4 d = {dppu<0x00>(e), dppu<0x55>(e), dppu<0xAA>(e), dppu<0xFF>(e)};
        stc128(rs, (lane & 3) == 0 ? blk + part * 256 + 4 * lane : OOB, d);
        LSTAMP(3);
      }
    }
#undef A_ROW
    PDUMP();
    return;
  }

  const int col = xcd * 4 + (slot & 3), bsel = slot >> 2;
  float* red = reinterpret_cast<float*>(smem + LH_RED);
  auto slice_load = [&](const u16* m, int cg) {
    const u16* sp = m + (size_t)(512 * wave + (lane >> 1)) * 512 + 16 * cg + 8 * (lane & 1);
#pragma unroll
    for (int i = 0; i < 16; ++i) {
      const int i2 = i ^ (4 * (slot & 3));
      glds16<0>(sp + (size_t)i2 * 32 * 512, lds0 + LH_WO + wave * 16384 + i2 * 1024);
    }
  };
  u32x4 wf[4][4];
  auto slice_frags = [&]() {
    LAS char* img = (LAS char*)(size_t)(lds0 + LH_WO + wave * 16384);
    const u32 q_ = (lane & 15) >> 2, pp = lane & 3;
#pragma unroll
    for (int hd = 0; hd < 4; ++hd)
#pragma unroll
      for (int ks = 0; ks < 4; ++ks) {
        s16x4 t[2];
#pragma unroll
        for (int tt = 0; tt < 2; ++tt)
          t[tt] = __builtin_amdgcn_ds_read_tr16_b64_v4i16((LAS s16x4*)(img + (128 * hd + 32 * ks + 8 * r + 4 * tt + q_) * 32 + 8 * pp));
        wf[hd][ks] = __builtin_bit_cast(u32x4, s16x8{t[0][0], t[0][1], t[0][2], t[0][3], t[1][0], t[1][1], t[1][2], t[1][3]});
      }
  };
  if (slot >= 4) {
    const int chunk = xcd * 4 + slot - 4, b = chunk >> 4;
    const u16* csrc = ckv + (size_t)chunk * 512 * DC;
    const rsrc_t rc = __builtin_amdgcn_make_buffer_rsrc(const_cast<u16*>(csrc), 0, 512 * DC * 2, 0x00020000);
    const int t0_ = 2 * (chunk & 3) + (xcd & 2);
    u32x4 stg[8];
    {
      u32x4 wv[16], xs[2][2];
#pragma unroll
      for (int i = 0; i < 4; ++i) xs[i >> 1][i & 1] = ld16(bx + (i >> 1) * DM + 64 * wave + 16 * r + 8 * (i & 1));
      if (tid < 2) stc64(rs, tid == 0 ? FL_B + (u32)chunk * 128 : WS_QB + (u32)chunk * 1024, u32x2{0u, 0u});
      asm volatile("" ::: "memory");
      const u16* wp = bwq + (size_t)(64 * wave + 16 * r) * 4096 + chunk * DH + 8 * c;
#pragma unroll
      for (int i = 0; i < 16; ++i) wv[i] = ld_nt(wp + (size_t)i * 4096);
      asm volatile("s_barrier" ::: "memory");
#pragma unroll
      for (int i = 0; i < 8; ++i) {
        const u32 row = 8 * wave + i;
        stg[i] = __builtin_amdgcn_raw_buffer_load_b128(rc, ((u32)lane ^ cswz(row & 15)) << 4, (64 * ((2 + t0_) & 7) + row) * 1024, AUX_NT);
      }
      float a0[8], a1[8];
#pragma unroll
      for (int k = 0; k < 8; ++k) { a0[k] = 0.f; a1[k] = 0.f; }
#pragma unroll
      for (int jj = 0; jj < 8; ++jj) {
        const u32 x0 = xs[0][jj >> 2][jj & 3], x1 = xs[1][jj >> 2][jj & 3];
#pragma unroll
        for (int k = 0; k < 4; ++k) {
          const u32 lo = perm_lo(wv[2 * jj][k], wv[2 * jj + 1][k]), hi = perm_hi(wv[2 * jj][k], wv[2 * jj + 1][k]);
          a0[2 * k] = dot2(lo, x0, a0[2 * k]); a0[2 * k + 1] = dot2(hi, x0, a0[2 * k + 1]);
          a1[2 * k] = dot2(lo, x1, a1[2 * k]); a1[2 * k + 1] = dot2(hi, x1, a1[2 * k + 1]);
        }
      }
#pragma unroll
      for (int k = 0; k < 8; ++k) { a0[k] = xrow16_sum(a0[k]); a1[k] = xrow16_sum(a1[k]); }
      float* red = reinterpret_cast<float*>(smem + LC_SP);
      if (r == 0) {
        *reinterpret_cast<f32x4*>(red + wave * 256 + 8 * c)           = f32x4{a0[0], a0[1], a0[2], a0[3]};
        *reinterpret_cast<f32x4*>(red + wave * 256 + 8 * c + 4)       = f32x4{a0[4], a0[5], a0[6], a0[7]};
        *reinterpret_cast<f32x4*>(red + wave * 256 + 128 + 8 * c)     = f32x4{a1[0], a1[1], a1[2], a1[3]};
        *reinterpret_cast<f32x4*>(red + wave * 256 + 128 + 8 * c + 4) = f32x4{a1[4], a1[5], a1[6], a1[7]};
      }
      bar();
      if (wave == 0) {
        f32x4 t = *reinterpret_cast<const f32x4*>(red + 4 * lane);
#pragma unroll
        for (int w = 1; w < 8; ++w) t += *reinterpret_cast<const f32x4*>(red + w * 256 + 4 * lane);
        const u32 v0 = pack2(t[0], t[1]), v1 = pack2(t[2], t[3]);
        stc128(rs, WS_QB + (u32)chunk * 1024 + 16 * lane, u32x4{v0, v0 ^ tag32, v1, v1 ^ tag32});
      }
      STAMP(1);
    }
#pragma unroll
    for (int st = 0; st < 2; ++st)
#pragma unroll
      for (int i = 0; i < 8; ++i) {
        const u32 row = 8 * wave + i;
        glds16<1>(csrc + (size_t)(64 * ((st + t0_) & 7) + row) * DC + 8 * ((u32)lane ^ cswz(row & 15)), lds0 + LC_RING + st * 65536 + row * 1024);
      }
    if (wave == 0) flags_wait<20>(ws, FL_Q, 4 * xcd + 1, tag, lane);
    bar();
    STAMP(5);
    const int kq = wave & 3, hq = wave >> 2;
    const u32 fsw = cswz((u32)c) << 4;
    u32x4 qh[16], ql[16];
    {
      u32x4 t[8];
#pragma unroll
      for (int i = 0; i < 8; ++i) t[i] = ldc128(rs, WS_QF + (u32)(b * 64 + 8 * wave) * 1024 + 16 * lane, (u32)i * 1024);
      const u32 qa = lds0 + LC_SP + (16 * hq + c) * 1024 + 256 * r + fsw;
#pragma unroll
      for (int hl = 0; hl < 2; ++hl) {
#pragma unroll
        for (int i = 0; i < 4; ++i) {
          const u32 row = 4 * wave + i;
          *(LAS u32x4*)(size_t)(lds0 + LC_SP + row * 1024 + (((u32)lane ^ cswz(row & 15)) << 4)) = t[2 * i + hl];
        }
        if (hl == 0) drain();
        bar();
#pragma unroll
        for (int s = 0; s < 16; ++s) (hl ? ql[s] : qh[s]) = *(const LAS u32x4*)(size_t)(qa ^ (u32)(s << 4));
        bar();
      }
    }
    STAMP(18);
    float mrun = -INFINITY, mgrun = -INFINITY, lsum = 0.f;
    f32x16 acc[2];
#pragma unroll
    for (int ct = 0; ct < 2; ++ct)
#pragma unroll
      for (int v = 0; v < 16; ++v) acc[ct][v] = 0.f;
    const u32 blk = (lane >> 4) & 1, q_ = (lane & 15) >> 2, pp = lane & 3;
    const u32 a1 = (16 * kq + c) * 1024 + 256 * r + fsw;
    float* mx = reinterpret_cast<float*>(smem + LC_MX);
#pragma unroll 1
    for (int st = 0; st < 8; ++st) {
      const u32 tb = lds0 + LC_RING + (st & 1) * 65536;
      f32x4 sa = {0.f, 0.f, 0.f, 0.f};
      {
        u32x4 af[4];
#pragma unroll
        for (int s = 0; s < 4; ++s) af[s] = *(const LAS u32x4*)(size_t)((tb + a1) ^ (u32)(s << 4));
#pragma unroll
        for (int s = 0; s < 16; ++s) {
          __builtin_amdgcn_sched_barrier(0);
          sa = mfma16(af[s & 3], qh[s], sa);
          sa = mfma16(af[s & 3], ql[s], sa);
          __builtin_amdgcn_sched_barrier(0);
          if (s + 4 < 16) af[s & 3] = *(const LAS u32x4*)(size_t)((tb + a1) ^ (u32)((s + 4) << 4));
        }
      }
      float sl[4];
#pragma unroll
      for (int v = 0; v < 4; ++v) sl[v] = score_log2(sa[v]);
      const float tm = xrow16_max(fmaxf(fmaxf(sl[0], sl[1]), fmaxf(sl[2], sl[3])));
      if (r == 0) mx[(16 * hq + c) * 4 + kq] = tm;
      bar();
      const f32x4 ms = *reinterpret_cast<const f32x4*>(mx + (16 * hq + c) * 4), mg = *reinterpret_cast<const f32x4*>(mx + l31 * 4);
      const float mn = fmaxf(mrun, fmaxf(fmaxf(ms[0], ms[1]), fmaxf(ms[2], ms[3])));
      const float mgn = fmaxf(mgrun, fmaxf(fmaxf(mg[0], mg[1]), fmaxf(mg[2], mg[3])));
      float ps = 0.f;
#pragma unroll
      for (int v = 0; v < 4; ++v) { sl[v] = __builtin_amdgcn_exp2f(sl[v] - mn); ps += sl[v]; }
      lsum = fmaf(lsum, __builtin_amdgcn_exp2f(mrun - mn), ps);
      mrun = mn;
      *reinterpret_cast<u32x2*>(smem + LC_P + (kq * 64 + (r & 1) * 32 + 16 * hq + c) * 16 + (r >> 1) * 8) =
          u32x2{pack2(sl[0], sl[1]), pack2(sl[2], sl[3])};
      const float alpha = __builtin_amdgcn_exp2f(mgrun - mgn);
      mgrun = mgn;
#pragma unroll
      for (int ct = 0; ct < 2; ++ct)
#pragma unroll
        for (int v = 0; v < 16; ++v) acc[ct][v] *= alpha;
      if (st >= 1 && st < 7) {
        const u32 ob = lds0 + LC_RING + ((st + 1) & 1) * 65536 + wave * 8192 + 16 * lane;
        const u32 so = st < 6 ? (u32)(64 * ((st + 2 + t0_) & 7) + 8 * wave) * 1024 : OOB;
#pragma unroll
        for (int i = 0; i < 8; ++i) {
          *(LAS u32x4*)(size_t)(ob + i * 1024) = stg[i];
          asm volatile("" ::: "memory");
          stg[i] = __builtin_amdgcn_raw_buffer_load_b128(rc, (((u32)lane ^ cswz((8 * wave + i) & 15)) << 4) + so + i * 1024, 0, AUX_NT);
        }
      }
      bar();
#pragma unroll
      for (int ks = 0; ks < 4; ++ks) {
        const u32x4 pf = *reinterpret_cast<const u32x4*>(smem + LC_P + (ks * 64 + lane) * 16);
#pragma unroll
        for (int ct = 0; ct < 2; ++ct) {
          s16x4 t[2];
#pragma unroll
          for (int tt = 0; tt < 2; ++tt) {
            const u32 row = 4 * hh + 8 * tt + q_;
            const u32 pc = 8 * wave + 4 * ct + 2 * blk + (pp >> 1);
            t[tt] = __builtin_amdgcn_ds_read_tr16_b64_v4i16((LAS s16x4*)(size_t)(tb + (16 * ks + row) * 1024 + ((pc ^ cswz(row)) << 4) + 8 * (pp & 1)));
          }
          const s16x8 cf = {t[0][0], t[0][1], t[0][2], t[0][3], t[1][0], t[1][1], t[1][2], t[1][3]};
          acc[ct] = __builtin_amdgcn_mfma_f32_32x32x16_bf16(__builtin_bit_cast(bf16x8, cf), __builtin_bit_cast(bf16x8, pf), acc[ct], 0, 0, 0);
        }
      }
    }
    STAMP(19);
    lsum = xrow16_sum(lsum);
    float* lsb = reinterpret_cast<float*>(smem + LC_LS);
    if (r == 0) lsb[kq * 32 + 16 * hq + c] = lsum;
#pragma unroll
    for (int ct = 0; ct < 2; ++ct)
#pragma unroll
      for (int v4 = 0; v4 < 4; ++v4)
        *reinterpret_cast<u32x2*>(smem + LC_RING + l31 * 1040 + (64 * wave + 32 * ct + 8 * v4 + 4 * hh) * 2) =
            u32x2{pack2(acc[ct][4 * v4], acc[ct][4 * v4 + 1]), pack2(acc[ct][4 * v4 + 2], acc[ct][4 * v4 + 3])};
    bar();
#pragma unroll
    for (int i = 0; i < 4; ++i) {
      const int hd = 8 * i + wave;
      __builtin_amdgcn_raw_buffer_store_b128(*reinterpret_cast<const u32x4*>(smem + LC_RING + hd * 1040 + 16 * lane), rs,
                                             WS_LATP + (u32)(chunk * 32 + hd) * 1024 + 16 * lane, 0, AUX_SC1 | AUX_NT);
    }
    if (tid < 32) stc64(rs, WS_LML + (u32)(chunk * 32 + tid) * 8,
                        u32x2{asu(mgrun), asu((lsb[tid] + lsb[32 + tid]) + (lsb[64 + tid] + lsb[96 + tid]))});
    if (tid < 2) reinterpret_cast<u32*>(smem + LH_W + 128)[tid] = 0u;
    drain();
    bar();
    if (tid == 0) flag_set(ws, FL_B + (u32)chunk * 128, tag);
    STAMP(3);
    slice_load(bwo, col ^ 16);
    drain();
    slice_frags();
#pragma unroll
    for (int hd = 0; hd < 4; ++hd) asm volatile("" : "+v"(wf[hd][0]), "+v"(wf[hd][1]), "+v"(wf[hd][2]), "+v"(wf[hd][3]) :: "memory");
    slice_load(wo, col);
    drain();
    STAMP(23);
  } else {

  const int h = xcd * 4 + slot, hx = 4 * slot;
  const u16* vp = wuv + (size_t)(64 * wave + r) * 4096 + h * DH + 8 * c;
  u32x4 wv[16];
  {
    u16* qv = reinterpret_cast<u16*>(smem + LH_Q);
    u16* qs = reinterpret_cast<u16*>(smem + LH_QS);
    u32x4 wk[16];
    {
      const u16* kp = wuk + (size_t)(64 * wave + r) * 4096 + h * DH + 8 * c;
#pragma unroll
      for (int i = 0; i < 16; ++i) wk[i] = ld_nt(kp + (size_t)(i ^ hx) * 4 * 4096);
      if (tid < (h ? 33 : 34)) stc128(rs, tid == 33 ? FL_LATE : tid == 32 ? FL_Q + (u32)h * 128 : WS_MA + (u32)(64 + h * 2 + r) * 1024 + 16 * c,
                           u32x4{VOID, VOID, VOID, VOID});
      if (tid < 2) reinterpret_cast<u32*>(smem + LH_W + 128)[tid] = 0u;
      asm volatile("" ::: "memory");
#pragma unroll
      for (int i = 0; i < 8; ++i) wv[i] = ld_nt(vp + (size_t)(i ^ hx) * 4 * 4096);
      asm volatile("" ::: "memory");
    }
    if (wave == 0) {
      u32 spins = 0;
      u32x4 g;
      for (;;) {
        g = ldc128(rs, WS_QB + (u32)h * 1024 + 16 * lane);
        if (__all(((g[0] ^ g[1] ^ tag32) | (g[2] ^ g[3] ^ tag32)) == 0u) || timed_out(ws, tag, spins, lane)) break;
        __builtin_amdgcn_s_sleep(4);
      }
      *reinterpret_cast<u32x2*>(qv + 4 * lane) = u32x2{g[0], g[2]};
    }
    bar();
#pragma unroll
    for (int bb = 0; bb < 2; ++bb) {
      const u32x4 q4 = ld16(qv + bb * 128 + 8 * c);
#pragma unroll
      for (int g = 0; g < 4; ++g) {
        float x[4];
#pragma unroll
        for (int t = 0; t < 4; ++t) {
          const u32x4 w4 = wk[4 * g + t];
          float d = dot2(w4[0], q4[0], 0.f);
          d = dot2(w4[1], q4[1], d); d = dot2(w4[2], q4[2], d); d = dot2(w4[3], q4[3], d);
          x[t] = d;
        }
        const float z = transpose_reduce4(x, c);
        if ((c & 3) == 0) {
          const int ci = 64 * wave + 4 * ((4 * g) ^ hx) + 4 * (c >> 2) + r;
          const u32 hi = bf16bits(z);
          qs[(bb * 2 + 0) * 512 + ci] = (u16)hi;
          qs[(bb * 2 + 1) * 512 + ci] = (u16)bf16bits(z - asf(hi << 16));
        }
      }
    }
    bar();
    if (tid < 256)
      stc128(rs, WS_QF + (u32)(((wave >> 1) * 32 + h) * 2 + (wave & 1)) * 1024 + 16 * lane, *reinterpret_cast<const u32x4*>(qs + 8 * tid));
    drain();
    bar();
    if (tid == 0) flag_set(ws, FL_Q + (u32)h * 128, tag);
    asm volatile("" ::: "memory");
#pragma unroll
    for (int i = 8; i < 16; ++i) wv[i] = ld_nt(vp + (size_t)(i ^ hx) * 4 * 4096);
    asm volatile("" ::: "memory");
    STAMP(1);
  }
  {
    slice_load(bwo, col ^ 16);
    drain();
    slice_frags();
#pragma unroll
    for (int hd = 0; hd < 4; ++hd) asm volatile("" : "+v"(wf[hd][0]), "+v"(wf[hd][1]), "+v"(wf[hd][2]), "+v"(wf[hd][3]) :: "memory");
    slice_load(wo, col);
    STAMP(17);
    {
      const rsrc_t rcode = __builtin_amdgcn_make_buffer_rsrc((void*)(pc0 & ~255ull), 0, 32768, 0x00020000);
      const u32x4 c0 = __builtin_amdgcn_raw_buffer_load_b128(rcode, (u32)(slot * 8 + wave) * 1024 + 16 * lane, 0, 0);
      asm volatile("" :: "v"(c0));
    }
    if (wave == 0) flags_wait<40>(ws, FL_B, h, tag, lane);
    bar();
    STAMP(2);
    float* cw = reinterpret_cast<float*>(smem + LH_W);
    float* part_ = reinterpret_cast<float*>(smem + LH_PART);
    float* lat = reinterpret_cast<float*>(smem + LH_LAT);
    u16* ctx = reinterpret_cast<u16*>(smem + LH_CTX);
    {
      const int bb = wave >> 2, qt = wave & 3;
      const u32 lo = WS_LATP + (u32)(4 * wave * 32 + h) * 1024 + 16 * lane;
      u32x4 v[4];
#pragma unroll
      for (int i = 0; i < 4; ++i) v[i] = __builtin_amdgcn_raw_buffer_load_b128(rs, lo, (u32)i * 32 * 1024, AUX_SC1 | AUX_NT);
      if (wave == 0) {
        const u32x2 ml = ldc64(rs, WS_LML + (u32)(l31 * 32 + h) * 8);
        const float mj = asf(ml[0]), lj = asf(ml[1]);
        const float M = row16_max(mj);
        const float w = __builtin_amdgcn_exp2f(mj - M);
        const float L = row16_sum(w * lj);
        cw[l31] = w / L;
      }
      bar();
      float a8[8];
#pragma unroll
      for (int e = 0; e < 8; ++e) a8[e] = 0.f;
#pragma unroll
      for (int i = 0; i < 4; ++i) {
        const float w = cw[4 * wave + i];
#pragma unroll
        for (int e = 0; e < 4; ++e) { a8[2 * e] = fmaf(w, bflo(v[i][e]), a8[2 * e]); a8[2 * e + 1] = fmaf(w, bfhi(v[i][e]), a8[2 * e + 1]); }
      }
      *reinterpret_cast<f32x4*>(part_ + (qt * 2 + bb) * 512 + 8 * lane)     = f32x4{a8[0], a8[1], a8[2], a8[3]};
      *reinterpret_cast<f32x4*>(part_ + (qt * 2 + bb) * 512 + 8 * lane + 4) = f32x4{a8[4], a8[5], a8[6], a8[7]};
      bar();
      lat[tid] = (part_[tid] + part_[1024 + tid]) + (part_[2048 + tid] + part_[3072 + tid]);
      lat[512 + tid] = (part_[512 + tid] + part_[1536 + tid]) + (part_[2560 + tid] + part_[3584 + tid]);
      bar();
    }
    {
      float a0[8], a1[8];
#pragma unroll
      for (int k = 0; k < 8; ++k) { a0[k] = 0.f; a1[k] = 0.f; }
#pragma unroll
      for (int i = 0; i < 16; ++i) {
        const float l0 = lat[64 * wave + 4 * (i ^ hx) + r], l1 = lat[512 + 64 * wave + 4 * (i ^ hx) + r];
#pragma unroll
        for (int k = 0; k < 4; ++k) {
          a0[2 * k] = fmaf(l0, bflo(wv[i][k]), a0[2 * k]); a0[2 * k + 1] = fmaf(l0, bfhi(wv[i][k]), a0[2 * k + 1]);
          a1[2 * k] = fmaf(l1, bflo(wv[i][k]), a1[2 * k]); a1[2 * k + 1] = fmaf(l1, bfhi(wv[i][k]), a1[2 * k + 1]);
        }
      }
#pragma unroll
      for (int k = 0; k < 8; ++k) { a0[k] = xrow16_sum(a0[k]); a1[k] = xrow16_sum(a1[k]); }
      if (r == 0) {
        *reinterpret_cast<f32x4*>(red + wave * 256 + 8 * c)           = f32x4{a0[0], a0[1], a0[2], a0[3]};
        *reinterpret_cast<f32x4*>(red + wave * 256 + 8 * c + 4)       = f32x4{a0[4], a0[5], a0[6], a0[7]};
        *reinterpret_cast<f32x4*>(red + wave * 256 + 128 + 8 * c)     = f32x4{a1[0], a1[1], a1[2], a1[3]};
        *reinterpret_cast<f32x4*>(red + wave * 256 + 128 + 8 * c + 4) = f32x4{a1[4], a1[5], a1[6], a1[7]};
      }
      bar();
      if (tid < 256) {
        float s = 0.f;
#pragma unroll
        for (int w = 0; w < 8; ++w) s += red[w * 256 + tid];
        ctx[tid] = (u16)bf16bits(s);
      }
      bar();
      if (tid < 32) {
        u32x4 d = ld16(ctx + 8 * tid);
#pragma unroll
        for (int k = 0; k < 4; ++k) d[k] = min(d[k], VOID - 1);
        stc128(rs, WS_MA + (u32)(64 + h * 2 + r) * 1024 + 16 * c, d);
      }
    }
    STAMP(4);
  }
  }
#pragma unroll 1
  for (int pa = 0; pa < 2; ++pa) {
    const bool skip = (c & 3) >= (pa ? 3 : 1);
    const u32 b0 = WS_MA + (u32)((pa ? 0 : 64) + 8 * wave + ((c >> 1) & 2) + bsel) * 1024;
    u32 spins = 0;
    float wt[2] = {1.f, 1.f};
    if (pa) {
      slice_frags();
      u32x4 hd[2];
      for (;;) {
        u32 bad = 0;
#pragma unroll
        for (int k = 0; k < 2; ++k) hd[k] = ldc128(rs, skip ? OOB : b0 + 768 + 16 * (c & 3), (u32)k * 4096);
#pragma unroll
        for (int k = 0; k < 2; ++k) bad |= (hd[k][3] ^ tag32) | (hd[k][0] ^ hd[k][1] ^ hd[k][2] ^ tag32);
        if (__all(skip || bad == 0u) || timed_out(ws, tag, spins, lane)) break;
        __builtin_amdgcn_s_sleep(16);
      }
#pragma unroll
      for (int k = 0; k < 2; ++k) {
        const float mm = skip ? -INFINITY : asf(hd[k][0]);
        float M = fmaxf(mm, dpp<XOR1>(mm)); M = fmaxf(M, dpp<XOR2>(M));
        const float ee = __builtin_amdgcn_exp2f(mm - M);
        float L = ee * asf(hd[k][1]); L += dpp<XOR1>(L); L += dpp<XOR2>(L);
        wt[k] = ee * __builtin_amdgcn_rcpf(L);
      }
    } else __builtin_amdgcn_s_sleep(32);
    const u32 od = b0 + (c & 3) * 256 + (c >> 3) * 64 + 16 * r;
    STAMP(20 + pa);
    u32x4 d[2][2];
    f32x4 e[2];
    bool miss[2];
#pragma unroll
    for (int k = 0; k < 2; ++k) { d[k][0] = d[k][1] = u32x4{0u, 0u, 0u, 0u}; e[k] = f32x4{0.f, 0.f, 0.f, 0.f}; miss[k] = !skip; }
    u32 pend = 3;
    for (;;) {
#pragma unroll
      for (int k = 0; k < 2; ++k)
        if (miss[k]) { d[k][0] = ldc128(rs, od, (u32)k * 4096); d[k][1] = ldc128(rs, od + 128, (u32)k * 4096); }
#pragma unroll
      for (int k = 0; k < 2; ++k) {
        if (!((pend >> k) & 1)) continue;
        miss[k] = max(max(max(d[k][0][0], d[k][0][1]), max(d[k][0][2], d[k][0][3])), max(max(d[k][1][0], d[k][1][1]), max(d[k][1][2], d[k][1][3]))) == VOID;
        if (__any(miss[k])) continue;
        pend &= ~(1u << k);
        const f32x4 z = {0.f, 0.f, 0.f, 0.f};
        f32x4 t[4];
#pragma unroll
        for (int q = 0; q < 4; ++q) t[q] = mfma16(wf[2 * k + (q >> 1)][q & 1], d[k][0], z);
#pragma unroll
        for (int q = 0; q < 4; ++q) t[q] = mfma16(wf[2 * k + (q >> 1)][2 + (q & 1)], d[k][1], t[q]);
#pragma unroll
        for (int v = 0; v < 4; ++v) e[k][v] = wt[k] * ((c & 4) ? ((c & 8) ? t[3][v] : t[2][v]) : ((c & 8) ? t[1][v] : t[0][v]));
      }
      if (pend == 0 || timed_out(ws, tag, spins, lane)) break;
      if (pa) __builtin_amdgcn_s_sleep(1); else __builtin_amdgcn_s_sleep(4);
    }
    WSTAMP(8);
    f32x4 y = e[0] + e[1];
#pragma unroll
    for (int v = 0; v < 4; ++v) y[v] = row16_sum(y[v]);
    float* fin = red + 128 * pa;
    if (c == 0) *reinterpret_cast<f32x4*>(fin + wave * 16 + 4 * r) = y;
    u32 n = 0;
    if (lane == 0) n = arrive_nw(lds0 + LH_W + 128 + 4 * pa);
    float t[8];
#pragma unroll
    for (int w = 0; w < 8; ++w) t[w] = fin[w * 16 + c];
    u32 o = bf16bits(((t[0] + t[1]) + (t[2] + t[3])) + ((t[4] + t[5]) + (t[6] + t[7])));
    asm volatile("" : "+v"(o), "+v"(n));
    if (__builtin_amdgcn_readfirstlane(n) == 7) {
      if (lane < 16) out[bsel * 1024 + (pa ? 16 * col : 512 + 16 * (col ^ 16)) + c] = (u16)o;
      LSTAMP(7);
    }
  }
}

extern "C" void kernel_launch(void* const* d_in, const int* in_sizes, int n_in, void* d_out, int out_size, void* d_ws, size_t ws_size,
                              hipStream_t stream) {
  mk_main<<<256, 512, 0, stream>>>((const u16*)d_in[0], (const u16*)d_in[1], (const u16*)d_in[2], (const u16*)d_in[3],
                                   (const u16*)d_in[4], (const u16*)d_in[5], (const u16*)d_in[6], (const u16*)d_in[7],
                                   (const u16*)d_in[8], (const u16*)d_in[9], (const u16*)d_in[10], (u16*)d_out, (char*)d_ws);
}
```
